```python
import torch

def _softmax(x, axis=-1):
    return torch.softmax(x, dim=axis)
def _gelu(x):
    return torch.nn.functional.gelu(x, approximate="tanh")
def _silu(x):
    return torch.nn.functional.silu(x)
def _sigmoid(x):
    return torch.sigmoid(x)
def _layernorm(x, axis=-1, eps=1e-5):
    m = x.mean(dim=axis, keepdim=True)
    v = x.var(dim=axis, keepdim=True, unbiased=False)
    return (x - m) / torch.sqrt(v + eps)
def _scatter_add(tgt, idx, src, axis=0):
    return tgt.clone().index_add_(axis, idx, src)
def _segment_sum(src, seg, n):
    out = torch.zeros((n,) + src.shape[1:], dtype=src.dtype)
    return out.index_add_(0, seg, src)
def _pad(x, pw):
    flat = tuple(v for pair in reversed(pw) for v in pair)
    return torch.nn.functional.pad(x, flat)

kernel_name = 'concat_heads__gqa_block_cached__mha_block_cached__near_ridge'

def setup_inputs():

    _w = (lambda a: (lambda t: t.to(torch.bfloat16) if t.is_floating_point() else t)(torch.as_tensor(a)))
    def _A_setup():
        B, Sq, D = 16, 48, 576
        Hq, Hkv, Sk, Dh = 32, 16, 48, 128
        assert Hq % Hkv == 0, (Hq, Hkv)
        return {
            "x": _w((lambda *s: torch.randn(*s, dtype=torch.bfloat16) * 0.8)(B, Sq, D)),
            "k_cache": _w((lambda *s: torch.randn(*s, dtype=torch.bfloat16) * 0.8)(B, Hkv, Sk, Dh)),
            "v_cache": _w((lambda *s: torch.randn(*s, dtype=torch.bfloat16) * 0.05)(B, Hkv, Sk, Dh)),
            "wq": _w((lambda *s: torch.randn(*s, dtype=torch.bfloat16) * 0.8)(D, Hq * Dh) * (D ** -0.5)),
            "wo": _w((lambda *s: torch.randn(*s, dtype=torch.bfloat16) * 0.05)(Hq * Dh, D) * ((Hq * Dh) ** -0.5)),
        }
    _A_in = _A_setup()

    def _B_setup():
        B, Sq, D = 16, 48, 576
        H, Sk, Dh = 32, 48, 128
        return {
            "x": _w((lambda *s: torch.randn(*s, dtype=torch.bfloat16) * 0.8)(B, Sq, D)),
            "k_cache": _w((lambda *s: torch.randn(*s, dtype=torch.bfloat16) * 0.8)(B, H, Sk, Dh)),
            "v_cache": _w((lambda *s: torch.randn(*s, dtype=torch.bfloat16) * 0.05)(B, H, Sk, Dh)),
            "wq": _w((lambda *s: torch.randn(*s, dtype=torch.bfloat16) * 0.8)(D, H * Dh) * (D ** -0.5)),
            "wo": _w((lambda *s: torch.randn(*s, dtype=torch.bfloat16) * 0.05)(H * Dh, D) * ((H * Dh) ** -0.5)),
        }
    _B_in = _B_setup()

    return {
        "A_x": _A_in["x"],
        "A_k_cache": _A_in["k_cache"],
        "A_v_cache": _A_in["v_cache"],
        "A_wq": _A_in["wq"],
        "A_wo": _A_in["wo"],
        "B_x": _B_in["x"],
        "B_k_cache": _B_in["k_cache"],
        "B_v_cache": _B_in["v_cache"],
        "B_wq": _B_in["wq"],
        "B_wo": _B_in["wo"],
    }

def reference(A_x, A_k_cache, A_v_cache, A_wq, A_wo, B_x, B_k_cache, B_v_cache, B_wq, B_wo):
    def _A_ref(x, k_cache, v_cache, wq, wo):
        B, Sq, D = x.shape
        Hq, Dh = 32, 128
        Hkv = k_cache.shape[1]
        rep = Hq // Hkv
        q = (lambda x, *d: x.permute(*d))((x @ wq).reshape(B, Sq, Hq, Dh), 0, 2, 1, 3)
        k = (lambda x, n, axis: torch.repeat_interleave(x, n, dim=axis))(k_cache, rep, axis=1)
        v = (lambda x, n, axis: torch.repeat_interleave(x, n, dim=axis))(v_cache, rep, axis=1)
        scores = torch.einsum("bhqd,bhkd->bhqk", q, k) * (Dh ** -0.5)
        p = _softmax(scores, axis=-1)
        o = torch.einsum("bhqk,bhkd->bhqd", p, v)
        return (lambda x, *d: x.permute(*d))(o, 0, 2, 1, 3).reshape(B, Sq, Hq * Dh) @ wo

    def _B_ref(x, k_cache, v_cache, wq, wo):
        B, Sq, D = x.shape
        H, Dh = 32, 128
        q = (lambda x, *d: x.permute(*d))((x @ wq).reshape(B, Sq, H, Dh), 0, 2, 1, 3)
        scores = torch.einsum("bhqd,bhkd->bhqk", q, k_cache) * (Dh ** -0.5)
        p = _softmax(scores, axis=-1)
        o = torch.einsum("bhqk,bhkd->bhqd", p, v_cache)
        return (lambda x, *d: x.permute(*d))(o, 0, 2, 1, 3).reshape(B, Sq, H * Dh) @ wo

    a = _A_ref(A_x, A_k_cache, A_v_cache, A_wq, A_wo)
    b = _B_ref(B_x, B_k_cache, B_v_cache, B_wq, B_wo)
    return torch.concatenate([a, b], axis=-1)
```

```cpp
#include <hip/hip_runtime.h>
#include <hip/hip_bf16.h>
typedef unsigned short u16;
__device__ __forceinline__ float bf2f(u16 x){ return __uint_as_float(((unsigned)x)<<16); }
__device__ __forceinline__ u16 f2bf(float f){ unsigned u=__float_as_uint(f); u+=0x7fffu+((u>>16)&1u); return (u16)(u>>16); }

__global__ void k_q(const u16* xA,const u16* wA,const u16* xB,const u16* wB,u16* q){
  int br=blockIdx.z; const u16* x=br?xB:xA; const u16* w=br?wB:wA;
  int n=blockIdx.x*256+threadIdx.x; int m=blockIdx.y;
  float acc=0; for(int k=0;k<576;++k) acc+=bf2f(x[m*576+k])*bf2f(w[(size_t)k*4096+n]);
  q[((size_t)br*768+m)*4096+n]=f2bf(acc);
}
__global__ void k_attn(const u16* q,const u16* kA,const u16* vA,const u16* kB,const u16* vB,u16* o){
  int br=blockIdx.z,b=blockIdx.y,h=blockIdx.x; int t=threadIdx.x; if(t>=48) return;
  const u16* K= br? kB+((size_t)(b*32+h))*48*128 : kA+((size_t)(b*16+h/2))*48*128;
  const u16* V= br? vB+((size_t)(b*32+h))*48*128 : vA+((size_t)(b*16+h/2))*48*128;
  const u16* qr=q+((size_t)br*768+b*48+t)*4096+h*128;
  __shared__ float s[48][49];
  float mx=-1e30f;
  for(int j=0;j<48;++j){ float a=0; for(int d=0;d<128;++d) a+=bf2f(qr[d])*bf2f(K[j*128+d]); a=bf2f(f2bf(a)); a=bf2f(f2bf(a*0.08838834764831845f)); s[t][j]=a; mx=fmaxf(mx,a);}
  float sum=0; for(int j=0;j<48;++j){ float e=__expf(s[t][j]-mx); s[t][j]=e; sum+=e; }
  for(int j=0;j<48;++j) s[t][j]=bf2f(f2bf(s[t][j]/sum));
  u16* orow=o+((size_t)br*768+b*48+t)*4096+h*128;
  for(int d=0;d<128;++d){ float a=0; for(int j=0;j<48;++j) a+=s[t][j]*bf2f(V[j*128+d]); orow[d]=f2bf(a);}
}
__global__ void k_o(const u16* o,const u16* wA,const u16* wB,u16* out){
  int br=blockIdx.z; const u16* w=br?wB:wA; int n=blockIdx.x*64+threadIdx.x; int m=blockIdx.y;
  const u16* orow=o+((size_t)br*768+m)*4096;
  float acc=0; for(int k=0;k<4096;++k) acc+=bf2f(orow[k])*bf2f(w[(size_t)k*576+n]);
  out[(size_t)m*1152+br*576+n]=f2bf(acc);
}
extern "C" void kernel_launch(void* const* d_in, const int* in_sizes, int n_in,
                              void* d_out, int out_size, void* d_ws, size_t ws_size,
                              hipStream_t stream) {
  const u16* Ax=(const u16*)d_in[0],*Ak=(const u16*)d_in[1],*Av=(const u16*)d_in[2],*Awq=(const u16*)d_in[3],*Awo=(const u16*)d_in[4];
  const u16* Bx=(const u16*)d_in[5],*Bk=(const u16*)d_in[6],*Bv=(const u16*)d_in[7],*Bwq=(const u16*)d_in[8],*Bwo=(const u16*)d_in[9];
  u16* q=(u16*)d_ws; u16* o=q+(size_t)2*768*4096;
  k_q<<<dim3(16,768,2),256,0,stream>>>(Ax,Awq,Bx,Bwq,q);
  k_attn<<<dim3(32,16,2),64,0,stream>>>(q,Ak,Av,Bk,Bv,o);
  k_o<<<dim3(9,768,2),64,0,stream>>>(o,Awo,Bwo,(u16*)d_out);
}
```

```cpp
#include <hip/hip_runtime.h>

__global__ __launch_bounds__(256, 1) void fused_attn_block_kernel(const void* a_x, const void* a_k, const void* a_v, const void* a_wq, const void* a_wo,
                                                              const void* b_x, const void* b_k, const void* b_v, const void* b_wq, const void* b_wo,
                                                              void* out, void* ws) {
  __shared__ __attribute__((aligned(16))) char lds[159744];
  asm volatile(
    "s_mov_b64 s[46:47], %0\n"
    "s_mov_b32 s52, %1\n"
    "s_bfe_u32 s54, s52, 0x10002\n"
    "s_mul_i32 s88, s54, 40\n"
    "s_add_u32 s90, s46, s88\n"
    "s_addc_u32 s91, s47, 0\n"
    "s_load_dwordx8 s[36:43], s[90:91], 0\n"
    "s_load_dwordx2 s[44:45], s[90:91], 32\n"
    "s_load_dwordx4 s[48:51], s[46:47], 80\n"
    "v_lshrrev_b32 v207, 6, %2\n"
    "v_and_b32 v202, 63, %2\n"
    "v_readfirstlane_b32 s53, v207\n"
    "v_and_b32 v203, 15, v202\n"
    "v_lshrrev_b32 v204, 4, v202\n"
    "s_bfe_u32 s93, s52, 0x30003\n"
    "s_lshr_b32 s61, s52, 6\n"
    "s_and_b32 s55, s52, 3\n"
    "s_lshl3_add_u32 s55, s55, s93\n"
    "s_lshl2_add_u32 s56, s61, s53\n"
    "s_mul_i32 s60, s53, 18432\n"
    "s_add_u32 s60, s60, 86016\n"
    "s_add_u32 s88, s61, s93\n"
    "s_mul_i32 s89, s88, 43\n"
    "s_lshr_b32 s89, s89, 7\n"
    "s_mul_i32 s89, s89, 3\n"
    "s_sub_u32 s88, s88, s89\n"
    "s_mul_i32 s88, s88, 3\n"
    "s_sub_u32 s89, s88, 9\n"
    "s_cselect_b32 s88, s88, s89\n"
    "s_lshl_b32 s83, s88, 7\n"
    "s_add_u32 s84, s83, 128\n"
    "s_cmp_eq_u32 s84, 1152\n"
    "s_cselect_b32 s84, 0, s84\n"
    "s_add_u32 s85, s84, 128\n"
    "s_cmp_eq_u32 s85, 1152\n"
    "s_cselect_b32 s85, 0, s85\n"
    "s_add_u32 s86, s85, 128\n"
    "s_cmp_eq_u32 s86, 1152\n"
    "s_cselect_b32 s86, 0, s86\n"
    "s_add_u32 s87, s86, 128\n"
    "s_cmp_eq_u32 s87, 1152\n"
    "s_cselect_b32 s87, 0, s87\n"
    "s_add_u32 s92, s87, 128\n"
    "s_cmp_eq_u32 s92, 1152\n"
    "s_cselect_b32 s92, 0, s92\n"
    "s_add_u32 s4, s92, 128\n"
    "s_cmp_eq_u32 s4, 1152\n"
    "s_cselect_b32 s4, 0, s4\n"
    "s_add_u32 s5, s4, 128\n"
    "s_cmp_eq_u32 s5, 1152\n"
    "s_cselect_b32 s5, 0, s5\n"
    "s_add_u32 s6, s5, 128\n"
    "s_cmp_eq_u32 s6, 1152\n"
    "s_cselect_b32 s6, 0, s6\n"
    "s_lshl_b32 s7, s53, 12\n"
    "s_lshl_b32 s88, s53, 17\n"
    "v_lshlrev_b32 v207, 1, v204\n"
    "v_xor_b32 v207, v207, v203\n"
    "v_lshlrev_b32 v207, 4, v207\n"
    "v_lshl_add_u32 v207, v204, 13, v207\n"
    "v_add_u32 v190, s88, v207\n"
    "v_add_u32 v191, 31744, v190\n"
    "v_xor_b32 v207, 0x80, v190\n"
    "v_add_u32 v192, 63488, v207\n"
    "v_add_u32 v193, 95232, v207\n"
    "v_lshrrev_b32 v205, 3, v202\n"
    "v_and_b32 v206, 7, v202\n"
    "v_xor_b32 v206, v206, v204\n"
    "v_lshlrev_b32 v206, 4, v206\n"
    "s_movk_i32 s88, 1152\n"
    "v_mad_u32_u24 v184, v205, s88, v206\n"
    "v_xor_b32 v207, 64, v184\n"
    "v_add_u32 v185, 8192, v207\n"
    "v_add_u32 v186, 16384, v184\n"
    "v_add_u32 v187, 27648, v207\n"
    "v_add_u32 v188, 35840, v184\n"
    "v_add_u32 v189, 44032, v207\n"
    "s_lshl_b32 s88, s55, 8\n"
    "s_mul_i32 s89, s56, 55296\n"
    "s_sub_u32 s22, 4718592, s88\n"
    "s_mov_b32 s23, 0x20000\n"
    "s_mov_b32 s10, 55296\n"
    "s_mov_b32 s11, 0x20000\n"
    "s_waitcnt lgkmcnt(0)\n"
    "s_add_u32 s20, s42, s88\n"
    "s_addc_u32 s21, s43, 0\n"
    "s_add_u32 s8, s36, s89\n"
    "s_addc_u32 s9, s37, 0\n"
    "s_lshl_b32 s62, s83, 12\n"
    "s_add_u32 m0, s7, 0\n"
    "s_nop 0\n"
    "buffer_load_dwordx4 v190, s[20:23], s62 offen offset:0 lds\n"
    "buffer_load_dwordx4 v191, s[20:23], s62 offen offset:1024 lds\n"
    "buffer_load_dwordx4 v192, s[20:23], s62 offen offset:2048 lds\n"
    "buffer_load_dwordx4 v193, s[20:23], s62 offen offset:3072 lds\n"
    "s_add_u32 m0, s60, 0\n"
    "s_nop 0\n"
    "buffer_load_dwordx4 v184, s[8:11], s83 offen offset:0 lds\n"
    "buffer_load_dwordx4 v185, s[8:11], s83 offen offset:1024 lds\n"
    "buffer_load_dwordx4 v186, s[8:11], s83 offen offset:2048 lds\n"
    "s_add_u32 m0, s60, 3072\n"
    "s_nop 0\n"
    "buffer_load_dwordx4 v187, s[8:11], s83 offen offset:0 lds\n"
    "buffer_load_dwordx4 v188, s[8:11], s83 offen offset:1024 lds\n"
    "buffer_load_dwordx4 v189, s[8:11], s83 offen offset:2048 lds\n"
    "s_getpc_b64 s[88:89]\n"
    "L_pc_%=:\n"
    "s_mov_b32 s90, L_end_%=-L_pc_%=\n"
    "s_mov_b32 s91, 0x20000\n"
    "s_lshr_b32 s82, s52, 3\n"
    "s_lshl2_add_u32 s82, s82, s53\n"
    "s_mul_i32 s82, s82, 384\n"
    "v_lshlrev_b32 v207, 7, v202\n"
    "v_cmp_gt_u32 vcc, 3, v202\n"
    "v_mov_b32 v208, 2147418112\n"
    "s_nop 0\n"
    "v_cndmask_b32 v207, v208, v207, vcc\n"
    "buffer_load_dword v248, v207, s[88:91], s82 offen offset:0\n"
    "s_mov_b32 s28, s50\n"
    "s_mov_b32 s29, s51\n"
    "s_mov_b32 s30, 19927040\n"
    "s_mov_b32 s31, 0x20000\n"
    "v_mov_b32 v240, 0\n"
    "buffer_load_dword v249, v240, s[28:31], 0 offen offset:0 sc1\n"
    "v_lshrrev_b32 v252, 2, v203\n"
    "v_and_b32 v253, 3, v203\n"
    "v_lshlrev_b32 v253, 3, v253\n"
    "v_lshl_add_u32 v250, v204, 3, v252\n"
    "v_and_b32 v251, 1, v204\n"
    "v_lshl_or_b32 v207, v251, 2, v252\n"
    "v_lshl_add_u32 v207, v207, 5, v253\n"
    "v_lshl_add_u32 v194, v250, 8, v207\n"
    "v_xor_b32 v195, 32, v194\n"
    "v_xor_b32 v196, 64, v194\n"
    "v_xor_b32 v197, 96, v194\n"
    "v_xor_b32 v198, 128, v194\n"
    "v_xor_b32 v199, 160, v194\n"
    "v_xor_b32 v200, 192, v194\n"
    "v_xor_b32 v201, 224, v194\n"
    "v_lshrrev_b32 v208, 1, v203\n"
    "v_lshl_add_u32 v209, v203, 7, s60\n"
    "v_xor_b32 v207, v204, v208\n"
    "v_lshl_add_u32 v231, v207, 4, v209\n"
    "v_xor_b32 v232, 64, v231\n"
    "s_add_u32 m0, s60, 6144\n"
    "s_nop 0\n"
    "buffer_load_dwordx4 v184, s[8:11], s84 offen offset:0 lds\n"
    "buffer_load_dwordx4 v185, s[8:11], s84 offen offset:1024 lds\n"
    "buffer_load_dwordx4 v186, s[8:11], s84 offen offset:2048 lds\n"
    "s_add_u32 m0, s60, 9216\n"
    "s_nop 0\n"
    "buffer_load_dwordx4 v187, s[8:11], s84 offen offset:0 lds\n"
    "buffer_load_dwordx4 v188, s[8:11], s84 offen offset:1024 lds\n"
    "buffer_load_dwordx4 v189, s[8:11], s84 offen offset:2048 lds\n"
    "s_lshl_b32 s62, s84, 12\n"
    "s_add_u32 m0, s7, 16384\n"
    "s_nop 0\n"
    "buffer_load_dwordx4 v190, s[20:23], s62 offen offset:0 lds\n"
    "buffer_load_dwordx4 v191, s[20:23], s62 offen offset:1024 lds\n"
    "buffer_load_dwordx4 v192, s[20:23], s62 offen offset:2048 lds\n"
    "buffer_load_dwordx4 v193, s[20:23], s62 offen offset:3072 lds\n"
    "s_waitcnt vmcnt(12)\n"
    "s_barrier\n"
    "ds_read_b128 a[132:135], v231 offset:0\n"
    "ds_read_b128 a[136:139], v231 offset:2048\n"
    "ds_read_b128 a[140:143], v231 offset:4096\n"
    "ds_read_b128 a[144:147], v232 offset:0\n"
    "ds_read_b128 a[148:151], v232 offset:2048\n"
    "ds_read_b128 a[152:155], v232 offset:4096\n"
    "ds_read_b64_tr_b16 a[108:109], v194 offset:0\n"
    "ds_read_b64_tr_b16 a[110:111], v194 offset:1024\n"
    "ds_read_b64_tr_b16 a[112:113], v195 offset:0\n"
    "ds_read_b64_tr_b16 a[114:115], v195 offset:1024\n"
    "ds_read_b64_tr_b16 a[116:117], v196 offset:0\n"
    "ds_read_b64_tr_b16 a[118:119], v196 offset:1024\n"
    "s_waitcnt lgkmcnt(4)\n"
    "v_mfma_f32_16x16x32_bf16 v[4:7], a[108:111], a[132:135], 0\n"
    "s_add_u32 m0, s60, 12288\n"
    "ds_read_b64_tr_b16 a[120:121], v197 offset:0\n"
    "buffer_load_dwordx4 v184, s[8:11], s85 offen offset:0 lds\n"
    "v_mfma_f32_16x16x32_bf16 v[8:11], a[108:111], a[136:139], 0\n"
    "s_lshr_b32 s57, s55, 2\n"
    "ds_read_b64_tr_b16 a[122:123], v197 offset:1024\n"
    "buffer_load_dwordx4 v185, s[8:11], s85 offen offset:1024 lds\n"
    "v_mfma_f32_16x16x32_bf16 v[12:15], a[108:111], a[140:143], 0\n"
    "s_and_b32 s58, s55, 3\n"
    "v_lshlrev_b32 v237, 4, v202\n"
    "v_lshlrev_b32 v238, 3, v202\n"
    "s_waitcnt lgkmcnt(4)\n"
    "v_mfma_f32_16x16x32_bf16 v[16:19], a[112:115], a[132:135], 0\n"
    "s_lshl2_add_u32 s59, s52, s53\n"
    "ds_read_b64_tr_b16 a[124:125], v198 offset:0\n"
    "buffer_load_dwordx4 v186, s[8:11], s85 offen offset:2048 lds\n"
    "v_mfma_f32_16x16x32_bf16 v[20:23], a[112:115], a[136:139], 0\n"
    "s_add_u32 m0, s60, 15360\n"
    "ds_read_b64_tr_b16 a[126:127], v198 offset:1024\n"
    "v_add_u32 v239, 4096, v237\n"
    "v_mfma_f32_16x16x32_bf16 v[24:27], a[112:115], a[140:143], 0\n"
    "s_lshl_b32 s59, s59, 7\n"
    "buffer_load_dwordx4 v187, s[8:11], s85 offen offset:0 lds\n"
    "v_add_u32 v238, 3072, v238\n"
    "s_waitcnt lgkmcnt(4)\n"
    "v_mfma_f32_16x16x32_bf16 v[28:31], a[116:119], a[132:135], 0\n"
    "s_xor_b32 s93, s54, 1\n"
    "ds_read_b64_tr_b16 a[128:129], v199 offset:0\n"
    "v_mov_b32 v207, 2147418112\n"
    "v_mfma_f32_16x16x32_bf16 v[32:35], a[116:119], a[136:139], 0\n"
    "s_lshl_b32 s90, s54, 5\n"
    "ds_read_b64_tr_b16 a[130:131], v199 offset:1024\n"
    "buffer_load_dwordx4 v188, s[8:11], s85 offen offset:1024 lds\n"
    "v_mfma_f32_16x16x32_bf16 v[36:39], a[116:119], a[140:143], 0\n"
    "s_waitcnt lgkmcnt(4)\n"
    "v_cmp_eq_u32 vcc, 0, v202\n"
    "v_lshl_add_u32 v220, v205, 8, v206\n"
    "v_mfma_f32_16x16x32_bf16 v[40:43], a[120:123], a[132:135], 0\n"
    "s_xor_b32 s88, s55, 0\n"
    "ds_read_b64_tr_b16 a[108:109], v200 offset:0\n"
    "buffer_load_dwordx4 v189, s[8:11], s85 offen offset:2048 lds\n"
    "v_mfma_f32_16x16x32_bf16 v[44:47], a[120:123], a[136:139], 0\n"
    "s_lshl_b32 s62, s85, 12\n"
    "ds_read_b64_tr_b16 a[110:111], v200 offset:1024\n"
    "v_cndmask_b32 v240, v207, v202, vcc\n"
    "v_mfma_f32_16x16x32_bf16 v[48:51], a[120:123], a[140:143], 0\n"
    "s_waitcnt lgkmcnt(4)\n"
    "v_xor_b32 v207, 64, v220\n"
    "v_add_u32 v221, 1024, v207\n"
    "v_mfma_f32_16x16x32_bf16 v[52:55], a[124:127], a[132:135], 0\n"
    "s_add_u32 m0, s7, 32768\n"
    "ds_read_b64_tr_b16 a[112:113], v201 offset:0\n"
    "buffer_load_dwordx4 v190, s[20:23], s62 offen offset:0 lds\n"
    "v_mfma_f32_16x16x32_bf16 v[56:59], a[124:127], a[136:139], 0\n"
    "s_mul_i32 s69, s88, 147456\n"
    "ds_read_b64_tr_b16 a[114:115], v201 offset:1024\n"
    "buffer_load_dwordx4 v191, s[20:23], s62 offen offset:1024 lds\n"
    "v_mfma_f32_16x16x32_bf16 v[60:63], a[124:127], a[140:143], 0\n"
    "s_waitcnt lgkmcnt(4)\n"
    "buffer_load_dwordx4 v192, s[20:23], s62 offen offset:2048 lds\n"
    "v_add_u32 v222, 2048, v220\n"
    "v_mfma_f32_16x16x32_bf16 v[64:67], a[128:131], a[132:135], 0\n"
    "s_lshr_b32 s89, s88, s93\n"
    "ds_read_b64_tr_b16 a[116:117], v194 offset:8192\n"
    "v_add_u32 v223, 6144, v207\n"
    "v_mfma_f32_16x16x32_bf16 v[68:71], a[128:131], a[136:139], 0\n"
    "s_mul_i32 s65, s89, 12288\n"
    "ds_read_b64_tr_b16 a[118:119], v194 offset:9216\n"
    "buffer_load_dwordx4 v193, s[20:23], s62 offen offset:3072 lds\n"
    "v_mfma_f32_16x16x32_bf16 v[72:75], a[128:131], a[140:143], 0\n"
    "s_waitcnt lgkmcnt(4)\n"
    "v_add_u32 v224, 7168, v220\n"
    "v_add_u32 v225, 8192, v207\n"
    "v_mfma_f32_16x16x32_bf16 v[76:79], a[108:111], a[132:135], 0\n"
    "s_waitcnt vmcnt(14)\n"
    "ds_read_b64_tr_b16 a[120:121], v195 offset:8192\n"
    "ds_read_b128 a[156:159], v231 offset:6144\n"
    "v_mfma_f32_16x16x32_bf16 v[80:83], a[108:111], a[136:139], 0\n"
    "s_add_u32 m0, s60, 0\n"
    "ds_read_b64_tr_b16 a[122:123], v195 offset:9216\n"
    "buffer_load_dwordx4 v184, s[8:11], s86 offen offset:0 lds\n"
    "v_mfma_f32_16x16x32_bf16 v[84:87], a[108:111], a[140:143], 0\n"
    "s_waitcnt lgkmcnt(5)\n"
    "ds_read_b128 a[160:163], v231 offset:8192\n"
    "buffer_load_dwordx4 v185, s[8:11], s86 offen offset:1024 lds\n"
    "v_mfma_f32_16x16x32_bf16 v[88:91], a[112:115], a[132:135], 0\n"
    "s_add_u32 s88, s88, s90\n"
    "s_lshl4_add_u32 s88, s88, s56\n"
    "ds_read_b64_tr_b16 a[124:125], v196 offset:8192\n"
    "v_mfma_f32_16x16x32_bf16 v[92:95], a[112:115], a[136:139], 0\n"
    "s_mul_i32 s88, s88, 4608\n"
    "ds_read_b64_tr_b16 a[126:127], v196 offset:9216\n"
    "ds_read_b128 a[164:167], v231 offset:10240\n"
    "v_mfma_f32_16x16x32_bf16 v[96:99], a[112:115], a[140:143], 0\n"
    "s_waitcnt lgkmcnt(7)\n"
    "s_add_u32 s73, s88, 528384\n"
    "buffer_load_dwordx4 v186, s[8:11], s86 offen offset:2048 lds\n"
    "v_mfma_f32_16x16x32_bf16 v[4:7], a[116:119], a[144:147], v[4:7]\n"
    "s_add_u32 m0, s60, 3072\n"
    "ds_read_b64_tr_b16 a[128:129], v197 offset:8192\n"
    "ds_read_b128 a[168:171], v232 offset:6144\n"
    "v_mfma_f32_16x16x32_bf16 v[8:11], a[116:119], a[148:151], v[8:11]\n"
    "s_lshl_b32 s88, s61, 1\n"
    "ds_read_b64_tr_b16 a[130:131], v197 offset:9216\n"
    "buffer_load_dwordx4 v187, s[8:11], s86 offen offset:0 lds\n"
    "v_mfma_f32_16x16x32_bf16 v[12:15], a[116:119], a[152:155], v[12:15]\n"
    "s_waitcnt lgkmcnt(7)\n"
    "ds_read_b128 a[172:175], v232 offset:8192\n"
    "buffer_load_dwordx4 v188, s[8:11], s86 offen offset:1024 lds\n"
    "v_mfma_f32_16x16x32_bf16 v[16:19], a[120:123], a[144:147], v[16:19]\n"
    "s_lshr_b32 s88, 165, s88\n"
    "ds_read_b64_tr_b16 a[108:109], v198 offset:8192\n"
    "buffer_load_dwordx4 v189, s[8:11], s86 offen offset:2048 lds\n"
    "v_mfma_f32_16x16x32_bf16 v[20:23], a[120:123], a[148:151], v[20:23]\n"
    "s_lshl_b32 s62, s86, 12\n"
    "ds_read_b64_tr_b16 a[110:111], v198 offset:9216\n"
    "ds_read_b128 a[176:179], v232 offset:10240\n"
    "v_mfma_f32_16x16x32_bf16 v[24:27], a[120:123], a[152:155], v[24:27]\n"
    "s_waitcnt lgkmcnt(8)\n"
    "s_add_u32 m0, s7, 49152\n"
    "s_nop 0\n"
    "buffer_load_dwordx4 v190, s[20:23], s62 offen offset:0 lds\n"
    "v_mfma_f32_16x16x32_bf16 v[28:31], a[124:127], a[144:147], v[28:31]\n"
    "s_and_b32 s88, s88, 3\n"
    "s_xor_b32 s88, s55, s88\n"
    "ds_read_b64_tr_b16 a[112:113], v199 offset:8192\n"
    "v_mfma_f32_16x16x32_bf16 v[32:35], a[124:127], a[148:151], v[32:35]\n"
    "s_mul_i32 s70, s88, 147456\n"
    "ds_read_b64_tr_b16 a[114:115], v199 offset:9216\n"
    "buffer_load_dwordx4 v191, s[20:23], s62 offen offset:1024 lds\n"
    "v_mfma_f32_16x16x32_bf16 v[36:39], a[124:127], a[152:155], v[36:39]\n"
    "s_waitcnt lgkmcnt(6)\n"
    "s_lshr_b32 s89, s88, s93\n"
    "s_mul_i32 s66, s89, 12288\n"
    "v_mfma_f32_16x16x32_bf16 v[40:43], a[128:131], a[144:147], v[40:43]\n"
    "s_add_u32 s88, s88, s90\n"
    "ds_read_b64_tr_b16 a[116:117], v200 offset:8192\n"
    "buffer_load_dwordx4 v192, s[20:23], s62 offen offset:2048 lds\n"
    "v_mfma_f32_16x16x32_bf16 v[44:47], a[128:131], a[148:151], v[44:47]\n"
    "s_lshl4_add_u32 s88, s88, s56\n"
    "s_mul_i32 s88, s88, 4608\n"
    "ds_read_b64_tr_b16 a[118:119], v200 offset:9216\n"
    "v_mfma_f32_16x16x32_bf16 v[48:51], a[128:131], a[152:155], v[48:51]\n"
    "s_waitcnt lgkmcnt(5)\n"
    "s_add_u32 s74, s88, 528384\n"
    "buffer_load_dwordx4 v193, s[20:23], s62 offen offset:3072 lds\n"
    "v_mfma_f32_16x16x32_bf16 v[52:55], a[108:111], a[144:147], v[52:55]\n"
    "s_lshl_b32 s88, s61, 1\n"
    "s_lshr_b32 s88, 90, s88\n"
    "ds_read_b64_tr_b16 a[120:121], v201 offset:8192\n"
    "v_mfma_f32_16x16x32_bf16 v[56:59], a[108:111], a[148:151], v[56:59]\n"
    "ds_read_b64_tr_b16 a[122:123], v201 offset:9216\n"
    "s_waitcnt vmcnt(20)\n"
    "s_barrier\n"
    "v_mfma_f32_16x16x32_bf16 v[60:63], a[108:111], a[152:155], v[60:63]\n"
    "s_waitcnt lgkmcnt(4)\n"
    "s_and_b32 s88, s88, 3\n"
    "s_xor_b32 s88, s55, s88\n"
    "v_mfma_f32_16x16x32_bf16 v[64:67], a[112:115], a[144:147], v[64:67]\n"
    "s_mul_i32 s71, s88, 147456\n"
    "s_lshr_b32 s89, s88, s93\n"
    "ds_read_b64_tr_b16 a[124:125], v194 offset:16384\n"
    "v_mfma_f32_16x16x32_bf16 v[68:71], a[112:115], a[148:151], v[68:71]\n"
    "s_mul_i32 s67, s89, 12288\n"
    "s_add_u32 s88, s88, s90\n"
    "ds_read_b64_tr_b16 a[126:127], v194 offset:17408\n"
    "v_mfma_f32_16x16x32_bf16 v[72:75], a[112:115], a[152:155], v[72:75]\n"
    "s_waitcnt lgkmcnt(4)\n"
    "s_lshl4_add_u32 s88, s88, s56\n"
    "s_mul_i32 s88, s88, 4608\n"
    "v_mfma_f32_16x16x32_bf16 v[76:79], a[116:119], a[144:147], v[76:79]\n"
    "s_add_u32 s75, s88, 528384\n"
    "s_lshl_b32 s88, s61, 1\n"
    "ds_read_b64_tr_b16 a[128:129], v195 offset:16384\n"
    "v_mfma_f32_16x16x32_bf16 v[80:83], a[116:119], a[148:151], v[80:83]\n"
    "s_lshr_b32 s88, 255, s88\n"
    "s_and_b32 s88, s88, 3\n"
    "ds_read_b64_tr_b16 a[130:131], v195 offset:17408\n"
    "v_mfma_f32_16x16x32_bf16 v[84:87], a[116:119], a[152:155], v[84:87]\n"
    "s_waitcnt lgkmcnt(4)\n"
    "s_xor_b32 s88, s55, s88\n"
    "s_mul_i32 s72, s88, 147456\n"
    "v_mfma_f32_16x16x32_bf16 v[88:91], a[120:123], a[144:147], v[88:91]\n"
    "s_lshr_b32 s89, s88, s93\n"
    "s_mul_i32 s68, s89, 12288\n"
    "ds_read_b64_tr_b16 a[108:109], v196 offset:16384\n"
    "v_mfma_f32_16x16x32_bf16 v[92:95], a[120:123], a[148:151], v[92:95]\n"
    "s_add_u32 s88, s88, s90\n"
    "s_lshl4_add_u32 s88, s88, s56\n"
    "ds_read_b64_tr_b16 a[110:111], v196 offset:17408\n"
    "v_mfma_f32_16x16x32_bf16 v[96:99], a[120:123], a[152:155], v[96:99]\n"
    "s_waitcnt lgkmcnt(4)\n"
    "s_mul_i32 s88, s88, 4608\n"
    "s_add_u32 s76, s88, 528384\n"
    "v_mfma_f32_16x16x32_bf16 v[4:7], a[124:127], a[156:159], v[4:7]\n"
    "s_waitcnt vmcnt(14)\n"
    "ds_read_b64_tr_b16 a[112:113], v197 offset:16384\n"
    "v_mfma_f32_16x16x32_bf16 v[8:11], a[124:127], a[160:163], v[8:11]\n"
    "ds_read_b64_tr_b16 a[114:115], v197 offset:17408\n"
    "ds_read_b128 a[180:183], v231 offset:12288\n"
    "v_mfma_f32_16x16x32_bf16 v[12:15], a[124:127], a[164:167], v[12:15]\n"
    "s_waitcnt lgkmcnt(5)\n"
    "s_add_u32 m0, s60, 6144\n"
    "ds_read_b128 a[184:187], v231 offset:14336\n"
    "v_mfma_f32_16x16x32_bf16 v[16:19], a[128:131], a[156:159], v[16:19]\n"
    "s_lshl4_add_u32 s88, s54, s56\n"
    "ds_read_b64_tr_b16 a[116:117], v198 offset:16384\n"
    "buffer_load_dwordx4 v184, s[8:11], s87 offen offset:0 lds\n"
    "v_mfma_f32_16x16x32_bf16 v[20:23], a[128:131], a[160:163], v[20:23]\n"
    "s_lshl2_add_u32 s88, s88, s58\n"
    "ds_read_b64_tr_b16 a[118:119], v198 offset:17408\n"
    "ds_read_b128 a[188:191], v231 offset:16384\n"
    "v_mfma_f32_16x16x32_bf16 v[24:27], a[128:131], a[164:167], v[24:27]\n"
    "s_waitcnt lgkmcnt(7)\n"
    "s_mul_i32 s88, s88, 114688\n"
    "buffer_load_dwordx4 v185, s[8:11], s87 offen offset:1024 lds\n"
    "v_mfma_f32_16x16x32_bf16 v[28:31], a[108:111], a[156:159], v[28:31]\n"
    "s_add_u32 s77, s88, 5246976\n"
    "ds_read_b64_tr_b16 a[120:121], v199 offset:16384\n"
    "ds_read_b128 a[192:195], v232 offset:12288\n"
    "v_mfma_f32_16x16x32_bf16 v[32:35], a[108:111], a[160:163], v[32:35]\n"
    "s_lshl_b32 s89, s57, 13\n"
    "s_add_u32 s78, s77, s89\n"
    "ds_read_b64_tr_b16 a[122:123], v199 offset:17408\n"
    "v_mfma_f32_16x16x32_bf16 v[36:39], a[108:111], a[164:167], v[36:39]\n"
    "s_waitcnt lgkmcnt(8)\n"
    "s_movk_i32 s96, 320\n"
    "ds_read_b128 a[196:199], v232 offset:14336\n"
    "v_mfma_f32_16x16x32_bf16 v[40:43], a[112:115], a[156:159], v[40:43]\n"
    "s_and_b32 s97, s55, 1\n"
    "ds_read_b64_tr_b16 a[124:125], v200 offset:16384\n"
    "buffer_load_dwordx4 v186, s[8:11], s87 offen offset:2048 lds\n"
    "v_mfma_f32_16x16x32_bf16 v[44:47], a[112:115], a[160:163], v[44:47]\n"
    "s_add_u32 s81, s97, 1\n"
    "ds_read_b64_tr_b16 a[126:127], v200 offset:17408\n"
    "ds_read_b128 a[200:203], v232 offset:16384\n"
    "v_mfma_f32_16x16x32_bf16 v[48:51], a[112:115], a[164:167], v[48:51]\n"
    "s_waitcnt lgkmcnt(8)\n"
    "v_mad_u32_u24 v250, v250, s96, v253\n"
    "v_xor_b32 v207, s97, v251\n"
    "v_mfma_f32_16x16x32_bf16 v[52:55], a[116:119], a[156:159], v[52:55]\n"
    "s_add_u32 m0, s60, 9216\n"
    "ds_read_b64_tr_b16 a[128:129], v201 offset:16384\n"
    "v_xor_b32 v234, s81, v251\n"
    "v_mfma_f32_16x16x32_bf16 v[56:59], a[116:119], a[160:163], v[56:59]\n"
    "ds_read_b64_tr_b16 a[130:131], v201 offset:17408\n"
    "buffer_load_dwordx4 v187, s[8:11], s87 offen offset:0 lds\n"
    "v_mfma_f32_16x16x32_bf16 v[60:63], a[116:119], a[164:167], v[60:63]\n"
    "s_waitcnt lgkmcnt(6)\n"
    "v_lshl_add_u32 v233, v207, 5, v250\n"
    "v_lshl_add_u32 v234, v234, 5, v250\n"
    "v_mfma_f32_16x16x32_bf16 v[64:67], a[120:123], a[156:159], v[64:67]\n"
    "s_cmp_lt_u32 s57, 4\n"
    "s_cselect_b32 s89, 10, 2\n"
    "ds_read_b64_tr_b16 a[108:109], v194 offset:24576\n"
    "v_mfma_f32_16x16x32_bf16 v[68:71], a[120:123], a[160:163], v[68:71]\n"
    "s_lshl_b32 s89, s89, 13\n"
    "ds_read_b64_tr_b16 a[110:111], v194 offset:25600\n"
    "v_add_u32 v235, 0x10000, v233\n"
    "v_mfma_f32_16x16x32_bf16 v[72:75], a[120:123], a[164:167], v[72:75]\n"
    "s_waitcnt lgkmcnt(5)\n"
    "v_subrev_u32 v234, 32, v234\n"
    "v_lshrrev_b32 v207, 1, v204\n"
    "v_mfma_f32_16x16x32_bf16 v[76:79], a[124:127], a[156:159], v[76:79]\n"
    "s_add_u32 s79, s78, s89\n"
    "ds_read_b64_tr_b16 a[112:113], v195 offset:24576\n"
    "buffer_load_dwordx4 v188, s[8:11], s87 offen offset:1024 lds\n"
    "v_mfma_f32_16x16x32_bf16 v[80:83], a[124:127], a[160:163], v[80:83]\n"
    "s_cmp_lt_u32 s57, 6\n"
    "s_cselect_b32 s78, s78, 2147418112\n"
    "ds_read_b64_tr_b16 a[114:115], v195 offset:25600\n"
    "v_mfma_f32_16x16x32_bf16 v[84:87], a[124:127], a[164:167], v[84:87]\n"
    "s_waitcnt lgkmcnt(4)\n"
    "v_add_u32 v236, 0x10000, v234\n"
    "v_xor_b32 v207, v207, v208\n"
    "v_mfma_f32_16x16x32_bf16 v[88:91], a[128:131], a[156:159], v[88:91]\n"
    "s_lshl_b32 s89, 196608, s54\n"
    "ds_read_b64_tr_b16 a[116:117], v196 offset:24576\n"
    "v_lshl_add_u32 v207, v207, 4, v209\n"
    "v_mfma_f32_16x16x32_bf16 v[92:95], a[128:131], a[160:163], v[92:95]\n"
    "s_mul_i32 s88, s56, s89\n"
    "ds_read_b64_tr_b16 a[118:119], v196 offset:25600\n"
    "buffer_load_dwordx4 v189, s[8:11], s87 offen offset:2048 lds\n"
    "v_mfma_f32_16x16x32_bf16 v[96:99], a[128:131], a[164:167], v[96:99]\n"
    "s_waitcnt lgkmcnt(4)\n"
    "v_lshl_add_u32 v250, v251, 3, v207\n"
    "v_xor_b32 v251, 32, v250\n"
    "v_mfma_f32_16x16x32_bf16 v[4:7], a[108:111], a[168:171], v[4:7]\n"
    "ds_read_b64_tr_b16 a[120:121], v197 offset:24576\n"
    "v_xor_b32 v252, 64, v250\n"
    "v_mfma_f32_16x16x32_bf16 v[8:11], a[108:111], a[172:175], v[8:11]\n"
    "ds_read_b64_tr_b16 a[122:123], v197 offset:25600\n"
    "v_xor_b32 v253, 96, v250\n"
    "v_mfma_f32_16x16x32_bf16 v[12:15], a[108:111], a[176:179], v[12:15]\n"
    "s_waitcnt lgkmcnt(4)\n"
    "s_waitcnt vmcnt(10)\n"
    "v_and_b32 v207, 3, v202\n"
    "v_mfma_f32_16x16x32_bf16 v[16:19], a[112:115], a[168:171], v[16:19]\n"
    "s_lshl2_add_u32 s96, s52, s53\n"
    "ds_read_b64_tr_b16 a[124:125], v198 offset:24576\n"
    "ds_read_b128 a[132:135], v231 offset:0\n"
    "v_mfma_f32_16x16x32_bf16 v[20:23], a[112:115], a[172:175], v[20:23]\n"
    "s_lshl_b32 s96, s96, 7\n"
    "ds_read_b64_tr_b16 a[126:127], v198 offset:25600\n"
    "ds_read_b128 a[136:139], v231 offset:2048\n"
    "v_mfma_f32_16x16x32_bf16 v[24:27], a[112:115], a[176:179], v[24:27]\n"
    "s_waitcnt lgkmcnt(6)\n"
    "v_lshlrev_b32 v207, 12, v207\n"
    "v_xor_b32 v207, s96, v207\n"
    "v_mfma_f32_16x16x32_bf16 v[28:31], a[116:119], a[168:171], v[28:31]\n"
    "ds_read_b64_tr_b16 a[128:129], v199 offset:24576\n"
    "ds_read_b128 a[140:143], v231 offset:4096\n"
    "v_mfma_f32_16x16x32_bf16 v[32:35], a[116:119], a[172:175], v[32:35]\n"
    "s_add_u32 s12, s38, s88\n"
    "s_addc_u32 s13, s39, 0\n"
    "ds_read_b64_tr_b16 a[130:131], v199 offset:25600\n"
    "v_mfma_f32_16x16x32_bf16 v[36:39], a[116:119], a[176:179], v[36:39]\n"
    "s_waitcnt lgkmcnt(7)\n"
    "s_mov_b32 s14, s89\n"
    "v_add_u32 v241, 4096, v207\n"
    "v_mfma_f32_16x16x32_bf16 v[40:43], a[120:123], a[168:171], v[40:43]\n"
    "s_mov_b32 s15, 0x20000\n"
    "ds_read_b64_tr_b16 a[108:109], v200 offset:24576\n"
    "ds_read_b128 a[144:147], v232 offset:0\n"
    "v_mfma_f32_16x16x32_bf16 v[44:47], a[120:123], a[172:175], v[44:47]\n"
    "s_add_u32 s16, s40, s88\n"
    "s_addc_u32 s17, s41, 0\n"
    "ds_read_b64_tr_b16 a[110:111], v200 offset:25600\n"
    "v_mfma_f32_16x16x32_bf16 v[48:51], a[120:123], a[176:179], v[48:51]\n"
    "s_waitcnt lgkmcnt(7)\n"
    "s_mov_b32 s18, s89\n"
    "s_mov_b32 s19, 0x20000\n"
    "v_mfma_f32_16x16x32_bf16 v[52:55], a[124:127], a[168:171], v[52:55]\n"
    "s_and_b32 s89, s58, 1\n"
    "ds_read_b64_tr_b16 a[112:113], v201 offset:24576\n"
    "ds_read_b128 a[148:151], v232 offset:2048\n"
    "v_mfma_f32_16x16x32_bf16 v[56:59], a[124:127], a[172:175], v[56:59]\n"
    "ds_read_b64_tr_b16 a[114:115], v201 offset:25600\n"
    "s_barrier\n"
    "v_mfma_f32_16x16x32_bf16 v[60:63], a[124:127], a[176:179], v[60:63]\n"
    "s_waitcnt lgkmcnt(6)\n"
    "s_lshl_b32 s62, s87, 12\n"
    "s_add_u32 m0, s7, 0\n"
    "v_mfma_f32_16x16x32_bf16 v[64:67], a[128:131], a[168:171], v[64:67]\n"
    "s_mul_i32 s88, s58, 288\n"
    "ds_read_b64_tr_b16 a[116:117], v194 offset:32768\n"
    "ds_read_b128 a[152:155], v232 offset:4096\n"
    "v_mfma_f32_16x16x32_bf16 v[68:71], a[128:131], a[172:175], v[68:71]\n"
    "s_lshl_b32 s89, s89, 5\n"
    "ds_read_b64_tr_b16 a[118:119], v194 offset:33792\n"
    "buffer_load_dwordx4 v190, s[20:23], s62 offen offset:0 lds\n"
    "v_mfma_f32_16x16x32_bf16 v[72:75], a[128:131], a[176:179], v[72:75]\n"
    "s_waitcnt lgkmcnt(6)\n"
    "s_sub_u32 s89, s88, s89\n"
    "buffer_load_dwordx4 v191, s[20:23], s62 offen offset:1024 lds\n"
    "v_mfma_f32_16x16x32_bf16 v[76:79], a[108:111], a[168:171], v[76:79]\n"
    "s_add_u32 s24, s44, s89\n"
    "s_addc_u32 s25, s45, 0\n"
    "ds_read_b64_tr_b16 a[120:121], v195 offset:32768\n"
    "v_mfma_f32_16x16x32_bf16 v[80:83], a[108:111], a[172:175], v[80:83]\n"
    "s_sub_u32 s26, 4718592, s89\n"
    "ds_read_b64_tr_b16 a[122:123], v195 offset:33792\n"
    "buffer_load_dwordx4 v192, s[20:23], s62 offen offset:2048 lds\n"
    "v_mfma_f32_16x16x32_bf16 v[84:87], a[108:111], a[176:179], v[84:87]\n"
    "s_waitcnt lgkmcnt(5)\n"
    "s_mov_b32 s27, 0x20000\n"
    "s_mul_i32 s89, s56, 110592\n"
    "v_mfma_f32_16x16x32_bf16 v[88:91], a[112:115], a[168:171], v[88:91]\n"
    "s_add_u32 s88, s88, s89\n"
    "ds_read_b64_tr_b16 a[124:125], v196 offset:32768\n"
    "buffer_load_dwordx4 v193, s[20:23], s62 offen offset:3072 lds\n"
    "v_mfma_f32_16x16x32_bf16 v[92:95], a[112:115], a[172:175], v[92:95]\n"
    "s_mul_i32 s89, s54, 1152\n"
    "s_add_u32 s88, s88, s89\n"
    "ds_read_b64_tr_b16 a[126:127], v196 offset:33792\n"
    "v_mfma_f32_16x16x32_bf16 v[96:99], a[112:115], a[176:179], v[96:99]\n"
    "s_waitcnt lgkmcnt(4)\n"
    "s_add_u32 s32, s48, s88\n"
    "s_addc_u32 s33, s49, 0\n"
    "v_mfma_f32_16x16x32_bf16 v[4:7], a[116:119], a[180:183], v[4:7]\n"
    "s_add_u32 m0, s60, 0\n"
    "ds_read_b64_tr_b16 a[128:129], v197 offset:32768\n"
    "v_mfma_f32_16x16x32_bf16 v[8:11], a[116:119], a[184:187], v[8:11]\n"
    "ds_read_b64_tr_b16 a[130:131], v197 offset:33792\n"
    "buffer_load_dwordx4 v184, s[8:11], s92 offen offset:0 lds\n"
    "v_mfma_f32_16x16x32_bf16 v[12:15], a[116:119], a[188:191], v[12:15]\n"
    "s_waitcnt lgkmcnt(4)\n"
    "s_sub_u32 s34, 1769472, s88\n"
    "s_mov_b32 s35, 0x20000\n"
    "v_mfma_f32_16x16x32_bf16 v[16:19], a[120:123], a[180:183], v[16:19]\n"
    "s_cmp_ge_u32 s61, 2\n"
    "s_cbranch_scc1 L_alt_%=\n"
    "s_mov_b32 s80, 1040356334\n"
    "ds_read_b64_tr_b16 a[108:109], v198 offset:32768\n"
    "buffer_load_dwordx4 v185, s[8:11], s92 offen offset:1024 lds\n"
    "v_mfma_f32_16x16x32_bf16 v[20:23], a[120:123], a[184:187], v[20:23]\n"
    "s_and_b32 s88, s61, 1\n"
    "s_lshl_b32 s98, s88, 7\n"
    "ds_read_b64_tr_b16 a[110:111], v198 offset:33792\n"
    "v_mfma_f32_16x16x32_bf16 v[24:27], a[120:123], a[188:191], v[24:27]\n"
    "s_waitcnt lgkmcnt(4)\n"
    "s_xor_b32 s99, s98, 128\n"
    "s_mul_i32 s46, s88, 73728\n"
    "v_mfma_f32_16x16x32_bf16 v[28:31], a[124:127], a[180:183], v[28:31]\n"
    "s_sub_u32 s47, 73728, s46\n"
    "ds_read_b64_tr_b16 a[112:113], v199 offset:32768\n"
    "buffer_load_dwordx4 v186, s[8:11], s92 offen offset:2048 lds\n"
    "v_mfma_f32_16x16x32_bf16 v[32:35], a[124:127], a[184:187], v[32:35]\n"
    "s_add_u32 m0, s60, 3072\n"
    "s_mul_i32 s50, s53, 5120\n"
    "ds_read_b64_tr_b16 a[114:115], v199 offset:33792\n"
    "v_mfma_f32_16x16x32_bf16 v[36:39], a[124:127], a[188:191], v[36:39]\n"
    "s_waitcnt lgkmcnt(4)\n"
    "s_and_b32 s97, s55, 3\n"
    "s_lshl3_add_u32 s96, s61, s97\n"
    "v_mfma_f32_16x16x32_bf16 v[40:43], a[128:131], a[180:183], v[40:43]\n"
    "s_lshl_b32 s96, s96, 3\n"
    "ds_read_b64_tr_b16 a[116:117], v200 offset:32768\n"
    "buffer_load_dwordx4 v187, s[8:11], s92 offen offset:0 lds\n"
    "v_mfma_f32_16x16x32_bf16 v[44:47], a[128:131], a[184:187], v[44:47]\n"
    "s_lshl2_add_u32 s96, s54, s96\n"
    "s_lshl2_add_u32 s96, s96, s53\n"
    "ds_read_b64_tr_b16 a[118:119], v200 offset:33792\n"
    "v_mfma_f32_16x16x32_bf16 v[48:51], a[128:131], a[188:191], v[48:51]\n"
    "s_waitcnt lgkmcnt(4)\n"
    "s_lshl_b32 s96, s96, 7\n"
    "s_add_u32 s96, s96, 135168\n"
    "v_mfma_f32_16x16x32_bf16 v[52:55], a[108:111], a[180:183], v[52:55]\n"
    "ds_read_b64_tr_b16 a[120:121], v201 offset:32768\n"
    "buffer_load_dwordx4 v188, s[8:11], s92 offen offset:1024 lds\n"
    "v_mfma_f32_16x16x32_bf16 v[56:59], a[108:111], a[184:187], v[56:59]\n"
    "ds_read_b64_tr_b16 a[122:123], v201 offset:33792\n"
    "v_and_b32 v207, 1, v202\n"
    "v_mfma_f32_16x16x32_bf16 v[60:63], a[108:111], a[188:191], v[60:63]\n"
    "s_waitcnt lgkmcnt(4)\n"
    "v_bfe_u32 v208, v202, 1, 2\n"
    "v_lshlrev_b32 v207, 14, v207\n"
    "v_mfma_f32_16x16x32_bf16 v[64:67], a[112:115], a[180:183], v[64:67]\n"
    "ds_read_b64_tr_b16 a[124:125], v194 offset:40960\n"
    "buffer_load_dwordx4 v189, s[8:11], s92 offen offset:2048 lds\n"
    "v_mfma_f32_16x16x32_bf16 v[68:71], a[112:115], a[184:187], v[68:71]\n"
    "ds_read_b64_tr_b16 a[126:127], v194 offset:41984\n"
    "v_lshl_add_u32 v207, v208, 9, v207\n"
    "v_mfma_f32_16x16x32_bf16 v[72:75], a[112:115], a[188:191], v[72:75]\n"
    "s_waitcnt lgkmcnt(4)\n"
    "v_add_u32 v242, s96, v207\n"
    "v_mfma_f32_16x16x32_bf16 v[76:79], a[116:119], a[180:183], v[76:79]\n"
    "s_add_u32 s63, s65, 0\n"
    "s_add_u32 m0, s60, 12288\n"
    "ds_read_b64_tr_b16 a[128:129], v195 offset:40960\n"
    "v_mfma_f32_16x16x32_bf16 v[80:83], a[116:119], a[184:187], v[80:83]\n"
    "s_movk_i32 s96, 2304\n"
    "ds_read_b64_tr_b16 a[130:131], v195 offset:41984\n"
    "buffer_load_dwordx4 v220, s[12:15], s63 offen offset:0 nt lds\n"
    "v_mfma_f32_16x16x32_bf16 v[84:87], a[116:119], a[188:191], v[84:87]\n"
    "s_waitcnt lgkmcnt(4)\n"
    "v_lshlrev_b32 v207, 3, v204\n"
    "v_mad_u32_u24 v243, v203, s96, v207\n"
    "v_mfma_f32_16x16x32_bf16 v[88:91], a[120:123], a[180:183], v[88:91]\n"
    "s_movk_i32 s96, 3277\n"
    "ds_read_b64_tr_b16 a[108:109], v196 offset:40960\n"
    "buffer_load_dwordx4 v221, s[12:15], s63 offen offset:1024 nt lds\n"
    "v_mfma_f32_16x16x32_bf16 v[92:95], a[120:123], a[184:187], v[92:95]\n"
    "s_movk_i32 s97, 1152\n"
    "s_mul_i32 s81, s53, 18432\n"
    "ds_read_b64_tr_b16 a[110:111], v196 offset:41984\n"
    "v_mfma_f32_16x16x32_bf16 v[96:99], a[120:123], a[188:191], v[96:99]\n"
    "s_waitcnt lgkmcnt(4)\n"
    "v_add_u32 v207, 0, v202\n"
    "v_mul_u32_u24 v208, s96, v207\n"
    "v_mfma_f32_16x16x32_bf16 v[4:7], a[124:127], a[192:195], v[4:7]\n"
    "ds_read_b64_tr_b16 a[112:113], v197 offset:40960\n"
    "buffer_load_dwordx4 v222, s[12:15], s63 offen offset:2048 nt lds\n"
    "v_mfma_f32_16x16x32_bf16 v[8:11], a[124:127], a[196:199], v[8:11]\n"
    "ds_read_b64_tr_b16 a[114:115], v197 offset:41984\n"
    "v_lshrrev_b32 v208, 16, v208\n"
    "v_mfma_f32_16x16x32_bf16 v[12:15], a[124:127], a[200:203], v[12:15]\n"
    "s_waitcnt lgkmcnt(4)\n"
    "v_mul_u32_u24 v226, 20, v208\n"
    "v_lshrrev_b32 v209, 2, v208\n"
    "v_mfma_f32_16x16x32_bf16 v[16:19], a[128:131], a[192:195], v[16:19]\n"
    "s_waitcnt vmcnt(13)\n"
    "ds_read_b64_tr_b16 a[116:117], v198 offset:40960\n"
    "ds_read_b128 a[156:159], v231 offset:6144\n"
    "v_mfma_f32_16x16x32_bf16 v[20:23], a[128:131], a[196:199], v[20:23]\n"
    "s_add_u32 m0, s60, 15360\n"
    "ds_read_b64_tr_b16 a[118:119], v198 offset:41984\n"
    "ds_read_b128 a[160:163], v231 offset:8192\n"
    "v_mfma_f32_16x16x32_bf16 v[24:27], a[128:131], a[200:203], v[24:27]\n"
    "s_waitcnt lgkmcnt(6)\n"
    "buffer_load_dwordx4 v223, s[12:15], s63 offen offset:0 nt lds\n"
    "v_sub_u32 v207, v207, v226\n"
    "v_mfma_f32_16x16x32_bf16 v[28:31], a[108:111], a[192:195], v[28:31]\n"
    "ds_read_b64_tr_b16 a[120:121], v199 offset:40960\n"
    "ds_read_b128 a[164:167], v231 offset:10240\n"
    "v_mfma_f32_16x16x32_bf16 v[32:35], a[108:111], a[196:199], v[32:35]\n"
    "ds_read_b64_tr_b16 a[122:123], v199 offset:41984\n"
    "buffer_load_dwordx4 v224, s[12:15], s63 offen offset:1024 nt lds\n"
    "v_mfma_f32_16x16x32_bf16 v[36:39], a[108:111], a[200:203], v[36:39]\n"
    "s_waitcnt lgkmcnt(7)\n"
    "v_and_b32 v209, 2, v209\n"
    "v_xor_b32 v207, v207, v209\n"
    "v_mfma_f32_16x16x32_bf16 v[40:43], a[112:115], a[192:195], v[40:43]\n"
    "ds_read_b64_tr_b16 a[124:125], v200 offset:40960\n"
    "ds_read_b128 a[168:171], v232 offset:6144\n"
    "v_mfma_f32_16x16x32_bf16 v[44:47], a[112:115], a[196:199], v[44:47]\n"
    "ds_read_b64_tr_b16 a[126:127], v200 offset:41984\n"
    "buffer_load_dwordx4 v225, s[12:15], s63 offen offset:2048 nt lds\n"
    "v_mfma_f32_16x16x32_bf16 v[48:51], a[112:115], a[200:203], v[48:51]\n"
    "s_waitcnt lgkmcnt(7)\n"
    "v_lshlrev_b32 v207, 4, v207\n"
    "v_mad_u32_u24 v207, v208, s97, v207\n"
    "v_mfma_f32_16x16x32_bf16 v[52:55], a[116:119], a[192:195], v[52:55]\n"
    "ds_read_b64_tr_b16 a[128:129], v201 offset:40960\n"
    "ds_read_b128 a[172:175], v232 offset:8192\n"
    "v_mfma_f32_16x16x32_bf16 v[56:59], a[116:119], a[196:199], v[56:59]\n"
    "ds_read_b64_tr_b16 a[130:131], v201 offset:41984\n"
    "s_barrier\n"
    "v_mfma_f32_16x16x32_bf16 v[60:63], a[116:119], a[200:203], v[60:63]\n"
    "s_waitcnt lgkmcnt(6)\n"
    "v_add_u32 v207, s81, v207\n"
    "v_subrev_u32 v226, 0, v207\n"
    "v_mfma_f32_16x16x32_bf16 v[64:67], a[120:123], a[192:195], v[64:67]\n"
    "s_lshl_b32 s62, s92, 12\n"
    "ds_read_b64_tr_b16 a[108:109], v194 offset:49152\n"
    "ds_read_b128 a[176:179], v232 offset:10240\n"
    "v_mfma_f32_16x16x32_bf16 v[68:71], a[120:123], a[196:199], v[68:71]\n"
    "s_add_u32 m0, s7, 16384\n"
    "ds_read_b64_tr_b16 a[110:111], v194 offset:50176\n"
    "buffer_load_dwordx4 v190, s[20:23], s62 offen offset:0 lds\n"
    "v_mfma_f32_16x16x32_bf16 v[72:75], a[120:123], a[200:203], v[72:75]\n"
    "s_waitcnt lgkmcnt(6)\n"
    "buffer_load_dwordx4 v191, s[20:23], s62 offen offset:1024 lds\n"
    "v_add_u32 v207, 64, v202\n"
    "v_mfma_f32_16x16x32_bf16 v[76:79], a[124:127], a[192:195], v[76:79]\n"
    "ds_read_b64_tr_b16 a[112:113], v195 offset:49152\n"
    "v_mul_u32_u24 v208, s96, v207\n"
    "v_mfma_f32_16x16x32_bf16 v[80:83], a[124:127], a[196:199], v[80:83]\n"
    "ds_read_b64_tr_b16 a[114:115], v195 offset:50176\n"
    "buffer_load_dwordx4 v192, s[20:23], s62 offen offset:2048 lds\n"
    "v_mfma_f32_16x16x32_bf16 v[84:87], a[124:127], a[200:203], v[84:87]\n"
    "s_waitcnt lgkmcnt(5)\n"
    "v_lshrrev_b32 v208, 16, v208\n"
    "v_mul_u32_u24 v227, 20, v208\n"
    "v_mfma_f32_16x16x32_bf16 v[88:91], a[128:131], a[192:195], v[88:91]\n"
    "ds_read_b64_tr_b16 a[116:117], v196 offset:49152\n"
    "buffer_load_dwordx4 v193, s[20:23], s62 offen offset:3072 lds\n"
    "v_mfma_f32_16x16x32_bf16 v[92:95], a[128:131], a[196:199], v[92:95]\n"
    "ds_read_b64_tr_b16 a[118:119], v196 offset:50176\n"
    "v_lshrrev_b32 v209, 2, v208\n"
    "v_mfma_f32_16x16x32_bf16 v[96:99], a[128:131], a[200:203], v[96:99]\n"
    "s_waitcnt lgkmcnt(4)\n"
    "v_sub_u32 v207, v207, v227\n"
    "v_and_b32 v209, 2, v209\n"
    "v_mfma_f32_16x16x32_bf16 v[4:7], a[108:111], a[132:135], v[4:7]\n"
    "ds_read_b64_tr_b16 a[120:121], v197 offset:49152\n"
    "v_xor_b32 v207, v207, v209\n"
    "v_mfma_f32_16x16x32_bf16 v[8:11], a[108:111], a[136:139], v[8:11]\n"
    "ds_read_b64_tr_b16 a[122:123], v197 offset:50176\n"
    "v_lshlrev_b32 v207, 4, v207\n"
    "v_mfma_f32_16x16x32_bf16 v[12:15], a[108:111], a[140:143], v[12:15]\n"
    "s_waitcnt lgkmcnt(4)\n"
    "v_mad_u32_u24 v207, v208, s97, v207\n"
    "v_add_u32 v207, s81, v207\n"
    "v_mfma_f32_16x16x32_bf16 v[16:19], a[112:115], a[132:135], v[16:19]\n"
    "s_add_u32 m0, s60, 6144\n"
    "ds_read_b64_tr_b16 a[124:125], v198 offset:49152\n"
    "buffer_load_dwordx4 v184, s[8:11], s4 offen offset:0 lds\n"
    "v_mfma_f32_16x16x32_bf16 v[20:23], a[112:115], a[136:139], v[20:23]\n"
    "ds_read_b64_tr_b16 a[126:127], v198 offset:50176\n"
    "buffer_load_dwordx4 v185, s[8:11], s4 offen offset:1024 lds\n"
    "v_mfma_f32_16x16x32_bf16 v[24:27], a[112:115], a[140:143], v[24:27]\n"
    "s_waitcnt lgkmcnt(4)\n"
    "v_subrev_u32 v227, 1024, v207\n"
    "v_add_u32 v207, 128, v202\n"
    "v_mfma_f32_16x16x32_bf16 v[28:31], a[116:119], a[132:135], v[28:31]\n"
    "ds_read_b64_tr_b16 a[128:129], v199 offset:49152\n"
    "buffer_load_dwordx4 v186, s[8:11], s4 offen offset:2048 lds\n"
    "v_mfma_f32_16x16x32_bf16 v[32:35], a[116:119], a[136:139], v[32:35]\n"
    "s_add_u32 m0, s60, 9216\n"
    "ds_read_b64_tr_b16 a[130:131], v199 offset:50176\n"
    "v_mul_u32_u24 v208, s96, v207\n"
    "v_mfma_f32_16x16x32_bf16 v[36:39], a[116:119], a[140:143], v[36:39]\n"
    "s_waitcnt lgkmcnt(4)\n"
    "v_lshrrev_b32 v208, 16, v208\n"
    "v_mul_u32_u24 v228, 20, v208\n"
    "v_mfma_f32_16x16x32_bf16 v[40:43], a[120:123], a[132:135], v[40:43]\n"
    "ds_read_b64_tr_b16 a[108:109], v200 offset:49152\n"
    "buffer_load_dwordx4 v187, s[8:11], s4 offen offset:0 lds\n"
    "v_mfma_f32_16x16x32_bf16 v[44:47], a[120:123], a[136:139], v[44:47]\n"
    "ds_read_b64_tr_b16 a[110:111], v200 offset:50176\n"
    "v_lshrrev_b32 v209, 2, v208\n"
    "v_mfma_f32_16x16x32_bf16 v[48:51], a[120:123], a[140:143], v[48:51]\n"
    "s_waitcnt lgkmcnt(4)\n"
    "v_sub_u32 v207, v207, v228\n"
    "v_and_b32 v209, 2, v209\n"
    "v_mfma_f32_16x16x32_bf16 v[52:55], a[124:127], a[132:135], v[52:55]\n"
    "ds_read_b64_tr_b16 a[112:113], v201 offset:49152\n"
    "buffer_load_dwordx4 v188, s[8:11], s4 offen offset:1024 lds\n"
    "v_xor_b32 v207, v207, v209\n"
    "v_lshlrev_b32 v207, 4, v207\n"
    "v_mad_u32_u24 v207, v208, s97, v207\n"
    "v_add_u32 v207, s81, v207\n"
    "v_subrev_u32 v228, 2048, v207\n"
    "v_add_u32 v207, 192, v202\n"
    "v_mul_u32_u24 v208, s96, v207\n"
    "v_lshrrev_b32 v208, 16, v208\n"
    "v_mul_u32_u24 v229, 20, v208\n"
    "v_lshrrev_b32 v209, 2, v208\n"
    "v_sub_u32 v207, v207, v229\n"
    "v_and_b32 v209, 2, v209\n"
    "v_xor_b32 v207, v207, v209\n"
    "v_lshlrev_b32 v207, 4, v207\n"
    "v_mad_u32_u24 v207, v208, s97, v207\n"
    "v_add_u32 v207, s81, v207\n"
    "v_subrev_u32 v229, 0, v207\n"
    "v_add_u32 v207, 256, v202\n"
    "v_mul_u32_u24 v208, s96, v207\n"
    "v_lshrrev_b32 v208, 16, v208\n"
    "v_mul_u32_u24 v230, 20, v208\n"
    "v_lshrrev_b32 v209, 2, v208\n"
    "v_sub_u32 v207, v207, v230\n"
    "v_and_b32 v209, 2, v209\n"
    "v_xor_b32 v207, v207, v209\n"
    "v_lshlrev_b32 v207, 4, v207\n"
    "v_mad_u32_u24 v207, v208, s97, v207\n"
    "v_add_u32 v207, s81, v207\n"
    "v_subrev_u32 v230, 1024, v207\n"
    "v_mfma_f32_16x16x32_bf16 v[56:59], a[124:127], a[136:139], v[56:59]\n"
    "ds_read_b64_tr_b16 a[114:115], v201 offset:50176\n"
    "v_mfma_f32_16x16x32_bf16 v[60:63], a[124:127], a[140:143], v[60:63]\n"
    "s_waitcnt lgkmcnt(4)\n"
    "v_mfma_f32_16x16x32_bf16 v[64:67], a[128:131], a[132:135], v[64:67]\n"
    "ds_read_b64_tr_b16 a[116:117], v194 offset:57344\n"
    "buffer_load_dwordx4 v189, s[8:11], s4 offen offset:2048 lds\n"
    "v_mfma_f32_16x16x32_bf16 v[68:71], a[128:131], a[136:139], v[68:71]\n"
    "ds_read_b64_tr_b16 a[118:119], v194 offset:58368\n"
    "v_mfma_f32_16x16x32_bf16 v[72:75], a[128:131], a[140:143], v[72:75]\n"
    "s_waitcnt lgkmcnt(4)\n"
    "v_mfma_f32_16x16x32_bf16 v[76:79], a[108:111], a[132:135], v[76:79]\n"
    "s_add_u32 s64, s69, s46\n"
    "s_add_u32 m0, s50, 65536\n"
    "ds_read_b64_tr_b16 a[120:121], v195 offset:57344\n"
    "v_mfma_f32_16x16x32_bf16 v[80:83], a[108:111], a[136:139], v[80:83]\n"
    "ds_read_b64_tr_b16 a[122:123], v195 offset:58368\n"
    "buffer_load_dwordx4 v226, s[24:27], s64 offen offset:0 lds\n"
    "v_mfma_f32_16x16x32_bf16 v[84:87], a[108:111], a[140:143], v[84:87]\n"
    "s_waitcnt lgkmcnt(4)\n"
    "v_mfma_f32_16x16x32_bf16 v[88:91], a[112:115], a[132:135], v[88:91]\n"
    "ds_read_b64_tr_b16 a[124:125], v196 offset:57344\n"
    "buffer_load_dwordx4 v227, s[24:27], s64 offen offset:1024 lds\n"
    "v_mfma_f32_16x16x32_bf16 v[92:95], a[112:115], a[136:139], v[92:95]\n"
    "ds_read_b64_tr_b16 a[126:127], v196 offset:58368\n"
    "v_mfma_f32_16x16x32_bf16 v[96:99], a[112:115], a[140:143], v[96:99]\n"
    "s_waitcnt lgkmcnt(4)\n"
    "v_mfma_f32_16x16x32_bf16 v[4:7], a[116:119], a[144:147], v[4:7]\n"
    "s_waitcnt vmcnt(18)\n"
    "ds_read_b64_tr_b16 a[128:129], v197 offset:57344\n"
    "v_mfma_f32_16x16x32_bf16 v[8:11], a[116:119], a[148:151], v[8:11]\n"
    "ds_read_b64_tr_b16 a[130:131], v197 offset:58368\n"
    "ds_read_b128 a[180:183], v231 offset:0\n"
    "v_mfma_f32_16x16x32_bf16 v[12:15], a[116:119], a[152:155], v[12:15]\n"
    "s_waitcnt lgkmcnt(5)\n"
    "buffer_load_dwordx4 v228, s[24:27], s64 offen offset:2048 lds\n"
    "v_mfma_f32_16x16x32_bf16 v[16:19], a[120:123], a[144:147], v[16:19]\n"
    "s_add_u32 m0, s50, 68608\n"
    "ds_read_b64_tr_b16 a[108:109], v198 offset:57344\n"
    "ds_read_b128 a[184:187], v231 offset:2048\n"
    "v_mfma_f32_16x16x32_bf16 v[20:23], a[120:123], a[148:151], v[20:23]\n"
    "ds_read_b64_tr_b16 a[110:111], v198 offset:58368\n"
    "v_mfma_f32_16x16x32_bf16 v[24:27], a[120:123], a[152:155], v[24:27]\n"
    "s_waitcnt lgkmcnt(6)\n"
    "buffer_load_dwordx4 v229, s[24:27], s64 offen offset:0 lds\n"
    "v_mfma_f32_16x16x32_bf16 v[28:31], a[124:127], a[144:147], v[28:31]\n"
    "ds_read_b64_tr_b16 a[112:113], v199 offset:57344\n"
    "ds_read_b128 a[188:191], v231 offset:4096\n"
    "v_mfma_f32_16x16x32_bf16 v[32:35], a[124:127], a[148:151], v[32:35]\n"
    "ds_read_b64_tr_b16 a[114:115], v199 offset:58368\n"
    "v_mfma_f32_16x16x32_bf16 v[36:39], a[124:127], a[152:155], v[36:39]\n"
    "s_waitcnt lgkmcnt(7)\n"
    "v_mfma_f32_16x16x32_bf16 v[40:43], a[128:131], a[144:147], v[40:43]\n"
    "ds_read_b64_tr_b16 a[116:117], v200 offset:57344\n"
    "ds_read_b128 a[192:195], v232 offset:0\n"
    "v_mfma_f32_16x16x32_bf16 v[44:47], a[128:131], a[148:151], v[44:47]\n"
    "ds_read_b64_tr_b16 a[118:119], v200 offset:58368\n"
    "buffer_load_dwordx4 v230, s[24:27], s64 offen offset:1024 lds\n"
    "v_mfma_f32_16x16x32_bf16 v[48:51], a[128:131], a[152:155], v[48:51]\n"
    "s_waitcnt lgkmcnt(6)\n"
    "v_mfma_f32_16x16x32_bf16 v[52:55], a[108:111], a[144:147], v[52:55]\n"
    "ds_read_b64_tr_b16 a[120:121], v201 offset:57344\n"
    "ds_read_b128 a[196:199], v232 offset:2048\n"
    "v_mfma_f32_16x16x32_bf16 v[56:59], a[108:111], a[148:151], v[56:59]\n"
    "ds_read_b64_tr_b16 a[122:123], v201 offset:58368\n"
    "s_barrier\n"
    "v_mfma_f32_16x16x32_bf16 v[60:63], a[108:111], a[152:155], v[60:63]\n"
    "s_waitcnt lgkmcnt(6)\n"
    "s_lshl_b32 s62, s4, 12\n"
    "s_add_u32 m0, s7, 32768\n"
    "v_mfma_f32_16x16x32_bf16 v[64:67], a[112:115], a[144:147], v[64:67]\n"
    "ds_read_b64_tr_b16 a[124:125], v194 offset:0\n"
    "ds_read_b128 a[200:203], v232 offset:4096\n"
    "v_mfma_f32_16x16x32_bf16 v[68:71], a[112:115], a[148:151], v[68:71]\n"
    "ds_read_b64_tr_b16 a[126:127], v194 offset:1024\n"
    "buffer_load_dwordx4 v190, s[20:23], s62 offen offset:0 lds\n"
    "v_mfma_f32_16x16x32_bf16 v[72:75], a[112:115], a[152:155], v[72:75]\n"
    "s_waitcnt lgkmcnt(6)\n"
    "buffer_load_dwordx4 v191, s[20:23], s62 offen offset:1024 lds\n"
    "v_mfma_f32_16x16x32_bf16 v[76:79], a[116:119], a[144:147], v[76:79]\n"
    "ds_read_b64_tr_b16 a[128:129], v195 offset:0\n"
    "v_mfma_f32_16x16x32_bf16 v[80:83], a[116:119], a[148:151], v[80:83]\n"
    "ds_read_b64_tr_b16 a[130:131], v195 offset:1024\n"
    "buffer_load_dwordx4 v192, s[20:23], s62 offen offset:2048 lds\n"
    "v_mfma_f32_16x16x32_bf16 v[84:87], a[116:119], a[152:155], v[84:87]\n"
    "s_waitcnt lgkmcnt(5)\n"
    "v_mfma_f32_16x16x32_bf16 v[88:91], a[120:123], a[144:147], v[88:91]\n"
    "ds_read_b64_tr_b16 a[108:109], v196 offset:0\n"
    "buffer_load_dwordx4 v193, s[20:23], s62 offen offset:3072 lds\n"
    "v_mfma_f32_16x16x32_bf16 v[92:95], a[120:123], a[148:151], v[92:95]\n"
    "ds_read_b64_tr_b16 a[110:111], v196 offset:1024\n"
    "v_mfma_f32_16x16x32_bf16 v[96:99], a[120:123], a[152:155], v[96:99]\n"
    "s_waitcnt lgkmcnt(4)\n"
    "v_mfma_f32_16x16x32_bf16 v[4:7], a[124:127], a[156:159], v[4:7]\n"
    "ds_read_b64_tr_b16 a[112:113], v197 offset:0\n"
    "v_add_u32 v249, 1, v249\n"
    "v_mfma_f32_16x16x32_bf16 v[8:11], a[124:127], a[160:163], v[8:11]\n"
    "s_waitcnt vmcnt(19)\n"
    "ds_read_b64_tr_b16 a[114:115], v197 offset:1024\n"
    "v_mfma_f32_16x16x32_bf16 v[12:15], a[124:127], a[164:167], v[12:15]\n"
    "s_waitcnt lgkmcnt(4)\n"
    "ds_read_b64 a[204:205], v250 offset:12288\n"
    "ds_read_b64 a[206:207], v251 offset:12288\n"
    "v_mfma_f32_16x16x32_bf16 v[16:19], a[128:131], a[156:159], v[16:19]\n"
    "s_add_u32 m0, s60, 0\n"
    "ds_read_b64_tr_b16 a[116:117], v198 offset:0\n"
    "ds_read_b64 a[208:209], v250 offset:14336\n"
    "v_mfma_f32_16x16x32_bf16 v[20:23], a[128:131], a[160:163], v[20:23]\n"
    "ds_read_b64_tr_b16 a[118:119], v198 offset:1024\n"
    "ds_read_b64 a[210:211], v251 offset:14336\n"
    "v_mfma_f32_16x16x32_bf16 v[24:27], a[128:131], a[164:167], v[24:27]\n"
    "s_waitcnt lgkmcnt(8)\n"
    "buffer_load_dwordx4 v184, s[8:11], s5 offen offset:0 lds\n"
    "buffer_load_dwordx4 v185, s[8:11], s5 offen offset:1024 lds\n"
    "v_mfma_f32_16x16x32_bf16 v[28:31], a[108:111], a[156:159], v[28:31]\n"
    "ds_read_b64_tr_b16 a[120:121], v199 offset:0\n"
    "ds_read_b64 a[212:213], v250 offset:16384\n"
    "v_mfma_f32_16x16x32_bf16 v[32:35], a[108:111], a[160:163], v[32:35]\n"
    "ds_read_b64_tr_b16 a[122:123], v199 offset:1024\n"
    "ds_read_b64 a[214:215], v251 offset:16384\n"
    "v_mfma_f32_16x16x32_bf16 v[36:39], a[108:111], a[164:167], v[36:39]\n"
    "s_waitcnt lgkmcnt(10)\n"
    "buffer_load_dwordx4 v186, s[8:11], s5 offen offset:2048 lds\n"
    "v_mfma_f32_16x16x32_bf16 v[40:43], a[112:115], a[156:159], v[40:43]\n"
    "s_add_u32 m0, s60, 3072\n"
    "ds_read_b64_tr_b16 a[124:125], v200 offset:0\n"
    "ds_read_b64 a[216:217], v252 offset:12288\n"
    "v_mfma_f32_16x16x32_bf16 v[44:47], a[112:115], a[160:163], v[44:47]\n"
    "ds_read_b64_tr_b16 a[126:127], v200 offset:1024\n"
    "ds_read_b64 a[218:219], v253 offset:12288\n"
    "v_mfma_f32_16x16x32_bf16 v[48:51], a[112:115], a[164:167], v[48:51]\n"
    "s_waitcnt lgkmcnt(9)\n"
    "buffer_load_dwordx4 v187, s[8:11], s5 offen offset:0 lds\n"
    "v_mfma_f32_16x16x32_bf16 v[52:55], a[116:119], a[156:159], v[52:55]\n"
    "ds_read_b64_tr_b16 a[128:129], v201 offset:0\n"
    "ds_read_b64 a[220:221], v252 offset:14336\n"
    "v_mfma_f32_16x16x32_bf16 v[56:59], a[116:119], a[160:163], v[56:59]\n"
    "ds_read_b64_tr_b16 a[130:131], v201 offset:1024\n"
    "ds_read_b64 a[222:223], v253 offset:14336\n"
    "v_mfma_f32_16x16x32_bf16 v[60:63], a[116:119], a[164:167], v[60:63]\n"
    "s_waitcnt lgkmcnt(9)\n"
    "buffer_load_dwordx4 v188, s[8:11], s5 offen offset:1024 lds\n"
    "v_mfma_f32_16x16x32_bf16 v[64:67], a[120:123], a[156:159], v[64:67]\n"
    "ds_read_b64_tr_b16 a[108:109], v194 offset:8192\n"
    "ds_read_b64 a[224:225], v252 offset:16384\n"
    "v_mfma_f32_16x16x32_bf16 v[68:71], a[120:123], a[160:163], v[68:71]\n"
    "ds_read_b64_tr_b16 a[110:111], v194 offset:9216\n"
    "ds_read_b64 a[226:227], v253 offset:16384\n"
    "v_mfma_f32_16x16x32_bf16 v[72:75], a[120:123], a[164:167], v[72:75]\n"
    "s_waitcnt lgkmcnt(9)\n"
    "buffer_load_dwordx4 v189, s[8:11], s5 offen offset:2048 lds\n"
    "v_mfma_f32_16x16x32_bf16 v[76:79], a[124:127], a[156:159], v[76:79]\n"
    "s_waitcnt lgkmcnt(0)\n"
    "s_add_u32 s63, s65, 128\n"
    "s_add_u32 m0, s60, 12288\n"
    "ds_read_b64_tr_b16 a[112:113], v195 offset:8192\n"
    "v_mfma_f32_16x16x32_bf16 v[80:83], a[124:127], a[160:163], v[80:83]\n"
    "ds_read_b64_tr_b16 a[114:115], v195 offset:9216\n"
    "buffer_load_dwordx4 v220, s[12:15], s63 offen offset:0 nt lds\n"
    "v_mfma_f32_16x16x32_bf16 v[84:87], a[124:127], a[164:167], v[84:87]\n"
    "v_mfma_f32_16x16x32_bf16 v[88:91], a[128:131], a[156:159], v[88:91]\n"
    "ds_read_b64_tr_b16 a[116:117], v196 offset:8192\n"
    "buffer_load_dwordx4 v221, s[12:15], s63 offen offset:1024 nt lds\n"
    "v_mfma_f32_16x16x32_bf16 v[92:95], a[128:131], a[160:163], v[92:95]\n"
    "ds_read_b64_tr_b16 a[118:119], v196 offset:9216\n"
    "v_mfma_f32_16x16x32_bf16 v[96:99], a[128:131], a[164:167], v[96:99]\n"
    "v_mfma_f32_16x16x32_bf16 v[4:7], a[108:111], a[168:171], v[4:7]\n"
    "ds_read_b64_tr_b16 a[120:121], v197 offset:8192\n"
    "buffer_load_dwordx4 v222, s[12:15], s63 offen offset:2048 nt lds\n"
    "v_mfma_f32_16x16x32_bf16 v[8:11], a[108:111], a[172:175], v[8:11]\n"
    "s_waitcnt vmcnt(18)\n"
    "ds_read_b64_tr_b16 a[122:123], v197 offset:9216\n"
    "v_mfma_f32_16x16x32_bf16 v[12:15], a[108:111], a[176:179], v[12:15]\n"
    "s_waitcnt lgkmcnt(4)\n"
    "s_add_u32 m0, s60, 15360\n"
    "ds_read_b128 a[132:135], v231 offset:6144\n"
    "v_mfma_f32_16x16x32_bf16 v[16:19], a[112:115], a[168:171], v[16:19]\n"
    "ds_read_b64_tr_b16 a[124:125], v198 offset:8192\n"
    "ds_read_b128 a[136:139], v231 offset:8192\n"
    "v_mfma_f32_16x16x32_bf16 v[20:23], a[112:115], a[172:175], v[20:23]\n"
    "ds_read_b64_tr_b16 a[126:127], v198 offset:9216\n"
    "buffer_load_dwordx4 v223, s[12:15], s63 offen offset:0 nt lds\n"
    "v_mfma_f32_16x16x32_bf16 v[24:27], a[112:115], a[176:179], v[24:27]\n"
    "s_waitcnt lgkmcnt(6)\n"
    "v_mfma_f32_16x16x32_bf16 v[28:31], a[116:119], a[168:171], v[28:31]\n"
    "ds_read_b64_tr_b16 a[128:129], v199 offset:8192\n"
    "ds_read_b128 a[140:143], v231 offset:10240\n"
    "v_mfma_f32_16x16x32_bf16 v[32:35], a[116:119], a[172:175], v[32:35]\n"
    "ds_read_b64_tr_b16 a[130:131], v199 offset:9216\n"
    "buffer_load_dwordx4 v224, s[12:15], s63 offen offset:1024 nt lds\n"
    "v_mfma_f32_16x16x32_bf16 v[36:39], a[116:119], a[176:179], v[36:39]\n"
    "s_waitcnt lgkmcnt(7)\n"
    "v_mfma_f32_16x16x32_bf16 v[40:43], a[120:123], a[168:171], v[40:43]\n"
    "ds_read_b64_tr_b16 a[108:109], v200 offset:8192\n"
    "ds_read_b128 a[144:147], v232 offset:6144\n"
    "v_mfma_f32_16x16x32_bf16 v[44:47], a[120:123], a[172:175], v[44:47]\n"
    "ds_read_b64_tr_b16 a[110:111], v200 offset:9216\n"
    "buffer_load_dwordx4 v225, s[12:15], s63 offen offset:2048 nt lds\n"
    "v_mfma_f32_16x16x32_bf16 v[48:51], a[120:123], a[176:179], v[48:51]\n"
    "s_waitcnt lgkmcnt(6)\n"
    "v_mfma_f32_16x16x32_bf16 v[52:55], a[124:127], a[168:171], v[52:55]\n"
    "ds_read_b64_tr_b16 a[112:113], v201 offset:8192\n"
    "ds_read_b128 a[148:151], v232 offset:8192\n"
    "v_mfma_f32_16x16x32_bf16 v[56:59], a[124:127], a[172:175], v[56:59]\n"
    "ds_read_b64_tr_b16 a[114:115], v201 offset:9216\n"
    "s_barrier\n"
    "v_mfma_f32_16x16x32_bf16 v[60:63], a[124:127], a[176:179], v[60:63]\n"
    "s_waitcnt lgkmcnt(6)\n"
    "s_lshl_b32 s62, s5, 12\n"
    "s_add_u32 m0, s7, 49152\n"
    "v_mfma_f32_16x16x32_bf16 v[64:67], a[128:131], a[168:171], v[64:67]\n"
    "ds_read_b64_tr_b16 a[116:117], v194 offset:16384\n"
    "ds_read_b128 a[152:155], v232 offset:10240\n"
    "v_mfma_f32_16x16x32_bf16 v[68:71], a[128:131], a[172:175], v[68:71]\n"
    "ds_read_b64_tr_b16 a[118:119], v194 offset:17408\n"
    "buffer_load_dwordx4 v190, s[20:23], s62 offen offset:0 lds\n"
    "v_mfma_f32_16x16x32_bf16 v[72:75], a[128:131], a[176:179], v[72:75]\n"
    "s_waitcnt lgkmcnt(6)\n"
    "buffer_load_dwordx4 v191, s[20:23], s62 offen offset:1024 lds\n"
    "v_mfma_f32_16x16x32_bf16 v[76:79], a[108:111], a[168:171], v[76:79]\n"
    "ds_read_b64_tr_b16 a[120:121], v195 offset:16384\n"
    "v_mfma_f32_16x16x32_bf16 v[80:83], a[108:111], a[172:175], v[80:83]\n"
    "ds_read_b64_tr_b16 a[122:123], v195 offset:17408\n"
    "buffer_load_dwordx4 v192, s[20:23], s62 offen offset:2048 lds\n"
    "v_mfma_f32_16x16x32_bf16 v[84:87], a[108:111], a[176:179], v[84:87]\n"
    "s_waitcnt lgkmcnt(5)\n"
    "v_mfma_f32_16x16x32_bf16 v[88:91], a[112:115], a[168:171], v[88:91]\n"
    "ds_read_b64_tr_b16 a[124:125], v196 offset:16384\n"
    "buffer_load_dwordx4 v193, s[20:23], s62 offen offset:3072 lds\n"
    "v_mfma_f32_16x16x32_bf16 v[92:95], a[112:115], a[172:175], v[92:95]\n"
    "ds_read_b64_tr_b16 a[126:127], v196 offset:17408\n"
    "v_mfma_f32_16x16x32_bf16 v[96:99], a[112:115], a[176:179], v[96:99]\n"
    "s_waitcnt lgkmcnt(4)\n"
    "v_mfma_f32_16x16x32_bf16 v[4:7], a[116:119], a[180:183], v[4:7]\n"
    "s_add_u32 m0, s60, 6144\n"
    "ds_read_b64_tr_b16 a[128:129], v197 offset:16384\n"
    "v_mfma_f32_16x16x32_bf16 v[8:11], a[116:119], a[184:187], v[8:11]\n"
    "ds_read_b64_tr_b16 a[130:131], v197 offset:17408\n"
    "buffer_load_dwordx4 v184, s[8:11], s6 offen offset:0 lds\n"
    "v_mfma_f32_16x16x32_bf16 v[12:15], a[116:119], a[188:191], v[12:15]\n"
    "s_waitcnt lgkmcnt(4)\n"
    "v_mfma_f32_16x16x32_bf16 v[16:19], a[120:123], a[180:183], v[16:19]\n"
    "ds_read_b64_tr_b16 a[108:109], v198 offset:16384\n"
    "v_mfma_f32_16x16x32_bf16 v[20:23], a[120:123], a[184:187], v[20:23]\n"
    "ds_read_b64_tr_b16 a[110:111], v198 offset:17408\n"
    "buffer_load_dwordx4 v185, s[8:11], s6 offen offset:1024 lds\n"
    "v_mfma_f32_16x16x32_bf16 v[24:27], a[120:123], a[188:191], v[24:27]\n"
    "s_waitcnt lgkmcnt(4)\n"
    "v_mfma_f32_16x16x32_bf16 v[28:31], a[124:127], a[180:183], v[28:31]\n"
    "ds_read_b64_tr_b16 a[112:113], v199 offset:16384\n"
    "v_mfma_f32_16x16x32_bf16 v[32:35], a[124:127], a[184:187], v[32:35]\n"
    "ds_read_b64_tr_b16 a[114:115], v199 offset:17408\n"
    "v_mfma_f32_16x16x32_bf16 v[36:39], a[124:127], a[188:191], v[36:39]\n"
    "s_waitcnt lgkmcnt(4)\n"
    "v_mfma_f32_16x16x32_bf16 v[40:43], a[128:131], a[180:183], v[40:43]\n"
    "ds_read_b64_tr_b16 a[116:117], v200 offset:16384\n"
    "buffer_load_dwordx4 v186, s[8:11], s6 offen offset:2048 lds\n"
    "v_mfma_f32_16x16x32_bf16 v[44:47], a[128:131], a[184:187], v[44:47]\n"
    "ds_read_b64_tr_b16 a[118:119], v200 offset:17408\n"
    "v_mfma_f32_16x16x32_bf16 v[48:51], a[128:131], a[188:191], v[48:51]\n"
    "s_waitcnt lgkmcnt(4)\n"
    "s_add_u32 m0, s60, 9216\n"
    "v_mfma_f32_16x16x32_bf16 v[52:55], a[108:111], a[180:183], v[52:55]\n"
    "ds_read_b64_tr_b16 a[120:121], v201 offset:16384\n"
    "v_mfma_f32_16x16x32_bf16 v[56:59], a[108:111], a[184:187], v[56:59]\n"
    "ds_read_b64_tr_b16 a[122:123], v201 offset:17408\n"
    "buffer_load_dwordx4 v187, s[8:11], s6 offen offset:0 lds\n"
    "v_mfma_f32_16x16x32_bf16 v[60:63], a[108:111], a[188:191], v[60:63]\n"
    "s_waitcnt lgkmcnt(4)\n"
    "v_mfma_f32_16x16x32_bf16 v[64:67], a[112:115], a[180:183], v[64:67]\n"
    "ds_read_b64_tr_b16 a[124:125], v194 offset:24576\n"
    "v_mfma_f32_16x16x32_bf16 v[68:71], a[112:115], a[184:187], v[68:71]\n"
    "ds_read_b64_tr_b16 a[126:127], v194 offset:25600\n"
    "v_mfma_f32_16x16x32_bf16 v[72:75], a[112:115], a[188:191], v[72:75]\n"
    "s_waitcnt lgkmcnt(4)\n"
    "v_mfma_f32_16x16x32_bf16 v[76:79], a[116:119], a[180:183], v[76:79]\n"
    "ds_read_b64_tr_b16 a[128:129], v195 offset:24576\n"
    "buffer_load_dwordx4 v188, s[8:11], s6 offen offset:1024 lds\n"
    "v_mfma_f32_16x16x32_bf16 v[80:83], a[116:119], a[184:187], v[80:83]\n"
    "ds_read_b64_tr_b16 a[130:131], v195 offset:25600\n"
    "v_mfma_f32_16x16x32_bf16 v[84:87], a[116:119], a[188:191], v[84:87]\n"
    "s_waitcnt lgkmcnt(4)\n"
    "v_mfma_f32_16x16x32_bf16 v[88:91], a[120:123], a[180:183], v[88:91]\n"
    "ds_read_b64_tr_b16 a[108:109], v196 offset:24576\n"
    "v_mfma_f32_16x16x32_bf16 v[92:95], a[120:123], a[184:187], v[92:95]\n"
    "ds_read_b64_tr_b16 a[110:111], v196 offset:25600\n"
    "buffer_load_dwordx4 v189, s[8:11], s6 offen offset:2048 lds\n"
    "v_mfma_f32_16x16x32_bf16 v[96:99], a[120:123], a[188:191], v[96:99]\n"
    "s_waitcnt lgkmcnt(4)\n"
    "v_mfma_f32_16x16x32_bf16 v[4:7], a[124:127], a[192:195], v[4:7]\n"
    "s_waitcnt vmcnt(16)\n"
    "ds_read_b64_tr_b16 a[112:113], v197 offset:24576\n"
    "v_mfma_f32_16x16x32_bf16 v[8:11], a[124:127], a[196:199], v[8:11]\n"
    "ds_read_b64_tr_b16 a[114:115], v197 offset:25600\n"
    "ds_read_b128 a[156:159], v231 offset:0\n"
    "v_mfma_f32_16x16x32_bf16 v[12:15], a[124:127], a[200:203], v[12:15]\n"
    "s_waitcnt lgkmcnt(5)\n"
    "v_mfma_f32_16x16x32_bf16 v[16:19], a[128:131], a[192:195], v[16:19]\n"
    "ds_read_b64_tr_b16 a[116:117], v198 offset:24576\n"
    "ds_read_b128 a[160:163], v231 offset:2048\n"
    "v_mfma_f32_16x16x32_bf16 v[20:23], a[128:131], a[196:199], v[20:23]\n"
    "ds_read_b64_tr_b16 a[118:119], v198 offset:25600\n"
    "v_mfma_f32_16x16x32_bf16 v[24:27], a[128:131], a[200:203], v[24:27]\n"
    "s_waitcnt lgkmcnt(6)\n"
    "v_mfma_f32_16x16x32_bf16 v[28:31], a[108:111], a[192:195], v[28:31]\n"
    "ds_read_b64_tr_b16 a[120:121], v199 offset:24576\n"
    "ds_read_b128 a[164:167], v231 offset:4096\n"
    "v_mfma_f32_16x16x32_bf16 v[32:35], a[108:111], a[196:199], v[32:35]\n"
    "ds_read_b64_tr_b16 a[122:123], v199 offset:25600\n"
    "v_mfma_f32_16x16x32_bf16 v[36:39], a[108:111], a[200:203], v[36:39]\n"
    "s_waitcnt lgkmcnt(7)\n"
    "v_mfma_f32_16x16x32_bf16 v[40:43], a[112:115], a[192:195], v[40:43]\n"
    "ds_read_b64_tr_b16 a[124:125], v200 offset:24576\n"
    "ds_read_b128 a[168:171], v232 offset:0\n"
    "v_mfma_f32_16x16x32_bf16 v[44:47], a[112:115], a[196:199], v[44:47]\n"
    "ds_read_b64_tr_b16 a[126:127], v200 offset:25600\n"
    "v_mfma_f32_16x16x32_bf16 v[48:51], a[112:115], a[200:203], v[48:51]\n"
    "s_waitcnt lgkmcnt(6)\n"
    "v_mfma_f32_16x16x32_bf16 v[52:55], a[116:119], a[192:195], v[52:55]\n"
    "ds_read_b64_tr_b16 a[128:129], v201 offset:24576\n"
    "ds_read_b128 a[172:175], v232 offset:2048\n"
    "v_mfma_f32_16x16x32_bf16 v[56:59], a[116:119], a[196:199], v[56:59]\n"
    "ds_read_b64_tr_b16 a[130:131], v201 offset:25600\n"
    "s_barrier\n"
    "v_mfma_f32_16x16x32_bf16 v[60:63], a[116:119], a[200:203], v[60:63]\n"
    "s_waitcnt lgkmcnt(6)\n"
    "s_lshl_b32 s62, s6, 12\n"
    "s_add_u32 m0, s7, 0\n"
    "v_mfma_f32_16x16x32_bf16 v[64:67], a[120:123], a[192:195], v[64:67]\n"
    "ds_read_b64_tr_b16 a[108:109], v194 offset:32768\n"
    "ds_read_b128 a[176:179], v232 offset:4096\n"
    "v_mfma_f32_16x16x32_bf16 v[68:71], a[120:123], a[196:199], v[68:71]\n"
    "ds_read_b64_tr_b16 a[110:111], v194 offset:33792\n"
    "buffer_load_dwordx4 v190, s[20:23], s62 offen offset:0 lds\n"
    "v_mfma_f32_16x16x32_bf16 v[72:75], a[120:123], a[200:203], v[72:75]\n"
    "s_waitcnt lgkmcnt(6)\n"
    "buffer_load_dwordx4 v191, s[20:23], s62 offen offset:1024 lds\n"
    "v_mfma_f32_16x16x32_bf16 v[76:79], a[124:127], a[192:195], v[76:79]\n"
    "ds_read_b64_tr_b16 a[112:113], v195 offset:32768\n"
    "v_mfma_f32_16x16x32_bf16 v[80:83], a[124:127], a[196:199], v[80:83]\n"
    "ds_read_b64_tr_b16 a[114:115], v195 offset:33792\n"
    "buffer_load_dwordx4 v192, s[20:23], s62 offen offset:2048 lds\n"
    "v_mfma_f32_16x16x32_bf16 v[84:87], a[124:127], a[200:203], v[84:87]\n"
    "s_waitcnt lgkmcnt(5)\n"
    "v_mfma_f32_16x16x32_bf16 v[88:91], a[128:131], a[192:195], v[88:91]\n"
    "ds_read_b64_tr_b16 a[116:117], v196 offset:32768\n"
    "buffer_load_dwordx4 v193, s[20:23], s62 offen offset:3072 lds\n"
    "v_mfma_f32_16x16x32_bf16 v[92:95], a[128:131], a[196:199], v[92:95]\n"
    "ds_read_b64_tr_b16 a[118:119], v196 offset:33792\n"
    "v_mfma_f32_16x16x32_bf16 v[96:99], a[128:131], a[200:203], v[96:99]\n"
    "s_waitcnt lgkmcnt(4)\n"
    "v_mfma_f32_16x16x32_bf16 v[4:7], a[108:111], a[132:135], v[4:7]\n"
    "s_waitcnt vmcnt(14)\n"
    "ds_read_b64_tr_b16 a[120:121], v197 offset:32768\n"
    "v_mfma_f32_16x16x32_bf16 v[8:11], a[108:111], a[136:139], v[8:11]\n"
    "ds_read_b64_tr_b16 a[122:123], v197 offset:33792\n"
    "ds_read_b64 a[228:229], v250 offset:12288\n"
    "v_mfma_f32_16x16x32_bf16 v[12:15], a[108:111], a[140:143], v[12:15]\n"
    "s_waitcnt lgkmcnt(5)\n"
    "ds_read_b64 a[230:231], v251 offset:12288\n"
    "v_mfma_f32_16x16x32_bf16 v[16:19], a[112:115], a[132:135], v[16:19]\n"
    "ds_read_b64_tr_b16 a[124:125], v198 offset:32768\n"
    "ds_read_b64 a[232:233], v250 offset:14336\n"
    "v_mfma_f32_16x16x32_bf16 v[20:23], a[112:115], a[136:139], v[20:23]\n"
    "ds_read_b64_tr_b16 a[126:127], v198 offset:33792\n"
    "ds_read_b64 a[234:235], v251 offset:14336\n"
    "v_mfma_f32_16x16x32_bf16 v[24:27], a[112:115], a[140:143], v[24:27]\n"
    "s_waitcnt lgkmcnt(8)\n"
    "v_mfma_f32_16x16x32_bf16 v[28:31], a[116:119], a[132:135], v[28:31]\n"
    "ds_read_b64_tr_b16 a[128:129], v199 offset:32768\n"
    "ds_read_b64 a[236:237], v250 offset:16384\n"
    "v_mfma_f32_16x16x32_bf16 v[32:35], a[116:119], a[136:139], v[32:35]\n"
    "ds_read_b64_tr_b16 a[130:131], v199 offset:33792\n"
    "ds_read_b64 a[238:239], v251 offset:16384\n"
    "v_mfma_f32_16x16x32_bf16 v[36:39], a[116:119], a[140:143], v[36:39]\n"
    "s_waitcnt lgkmcnt(10)\n"
    "v_mfma_f32_16x16x32_bf16 v[40:43], a[120:123], a[132:135], v[40:43]\n"
    "ds_read_b64_tr_b16 a[108:109], v200 offset:32768\n"
    "ds_read_b64 a[240:241], v252 offset:12288\n"
    "v_mfma_f32_16x16x32_bf16 v[44:47], a[120:123], a[136:139], v[44:47]\n"
    "ds_read_b64_tr_b16 a[110:111], v200 offset:33792\n"
    "ds_read_b64 a[242:243], v253 offset:12288\n"
    "v_mfma_f32_16x16x32_bf16 v[48:51], a[120:123], a[140:143], v[48:51]\n"
    "s_waitcnt lgkmcnt(9)\n"
    "v_mfma_f32_16x16x32_bf16 v[52:55], a[124:127], a[132:135], v[52:55]\n"
    "ds_read_b64_tr_b16 a[112:113], v201 offset:32768\n"
    "ds_read_b64 a[244:245], v252 offset:14336\n"
    "v_mfma_f32_16x16x32_bf16 v[56:59], a[124:127], a[136:139], v[56:59]\n"
    "ds_read_b64_tr_b16 a[114:115], v201 offset:33792\n"
    "ds_read_b64 a[246:247], v253 offset:14336\n"
    "v_mfma_f32_16x16x32_bf16 v[60:63], a[124:127], a[140:143], v[60:63]\n"
    "s_waitcnt lgkmcnt(9)\n"
    "v_mfma_f32_16x16x32_bf16 v[64:67], a[128:131], a[132:135], v[64:67]\n"
    "ds_read_b64_tr_b16 a[116:117], v194 offset:40960\n"
    "ds_read_b64 a[248:249], v252 offset:16384\n"
    "v_mfma_f32_16x16x32_bf16 v[68:71], a[128:131], a[136:139], v[68:71]\n"
    "ds_read_b64_tr_b16 a[118:119], v194 offset:41984\n"
    "ds_read_b64 a[250:251], v253 offset:16384\n"
    "v_mfma_f32_16x16x32_bf16 v[72:75], a[128:131], a[140:143], v[72:75]\n"
    "s_waitcnt lgkmcnt(9)\n"
    "v_mfma_f32_16x16x32_bf16 v[76:79], a[108:111], a[132:135], v[76:79]\n"
    "s_waitcnt lgkmcnt(0)\n"
    "s_add_u32 s63, s65, s98\n"
    "s_add_u32 m0, s60, 12288\n"
    "ds_read_b64_tr_b16 a[120:121], v195 offset:40960\n"
    "v_mfma_f32_16x16x32_bf16 v[80:83], a[108:111], a[136:139], v[80:83]\n"
    "ds_read_b64_tr_b16 a[122:123], v195 offset:41984\n"
    "buffer_load_dwordx4 v220, s[16:19], s63 offen offset:0 lds\n"
    "v_mfma_f32_16x16x32_bf16 v[84:87], a[108:111], a[140:143], v[84:87]\n"
    "v_mfma_f32_16x16x32_bf16 v[88:91], a[112:115], a[132:135], v[88:91]\n"
    "ds_read_b64_tr_b16 a[124:125], v196 offset:40960\n"
    "buffer_load_dwordx4 v221, s[16:19], s63 offen offset:1024 lds\n"
    "v_mfma_f32_16x16x32_bf16 v[92:95], a[112:115], a[136:139], v[92:95]\n"
    "ds_read_b64_tr_b16 a[126:127], v196 offset:41984\n"
    "v_mfma_f32_16x16x32_bf16 v[96:99], a[112:115], a[140:143], v[96:99]\n"
    "v_mfma_f32_16x16x32_bf16 v[4:7], a[116:119], a[144:147], v[4:7]\n"
    "ds_read_b64_tr_b16 a[128:129], v197 offset:40960\n"
    "buffer_load_dwordx4 v222, s[16:19], s63 offen offset:2048 lds\n"
    "v_mfma_f32_16x16x32_bf16 v[8:11], a[116:119], a[148:151], v[8:11]\n"
    "s_waitcnt vmcnt(7)\n"
    "ds_read_b64_tr_b16 a[130:131], v197 offset:41984\n"
    "v_mfma_f32_16x16x32_bf16 v[12:15], a[116:119], a[152:155], v[12:15]\n"
    "s_waitcnt lgkmcnt(4)\n"
    "s_add_u32 m0, s60, 15360\n"
    "ds_read_b128 a[180:183], v231 offset:6144\n"
    "v_mfma_f32_16x16x32_bf16 v[16:19], a[120:123], a[144:147], v[16:19]\n"
    "ds_read_b64_tr_b16 a[108:109], v198 offset:40960\n"
    "ds_read_b128 a[184:187], v231 offset:8192\n"
    "v_mfma_f32_16x16x32_bf16 v[20:23], a[120:123], a[148:151], v[20:23]\n"
    "ds_read_b64_tr_b16 a[110:111], v198 offset:41984\n"
    "buffer_load_dwordx4 v223, s[16:19], s63 offen offset:0 lds\n"
    "v_mfma_f32_16x16x32_bf16 v[24:27], a[120:123], a[152:155], v[24:27]\n"
    "s_waitcnt lgkmcnt(6)\n"
    "v_mfma_f32_16x16x32_bf16 v[28:31], a[124:127], a[144:147], v[28:31]\n"
    "ds_read_b64_tr_b16 a[112:113], v199 offset:40960\n"
    "ds_read_b128 a[188:191], v231 offset:10240\n"
    "v_mfma_f32_16x16x32_bf16 v[32:35], a[124:127], a[148:151], v[32:35]\n"
    "ds_read_b64_tr_b16 a[114:115], v199 offset:41984\n"
    "buffer_load_dwordx4 v224, s[16:19], s63 offen offset:1024 lds\n"
    "v_mfma_f32_16x16x32_bf16 v[36:39], a[124:127], a[152:155], v[36:39]\n"
    "s_waitcnt lgkmcnt(7)\n"
    "v_mfma_f32_16x16x32_bf16 v[40:43], a[128:131], a[144:147], v[40:43]\n"
    "ds_read_b64_tr_b16 a[116:117], v200 offset:40960\n"
    "ds_read_b128 a[192:195], v232 offset:6144\n"
    "v_mfma_f32_16x16x32_bf16 v[44:47], a[128:131], a[148:151], v[44:47]\n"
    "ds_read_b64_tr_b16 a[118:119], v200 offset:41984\n"
    "buffer_load_dwordx4 v225, s[16:19], s63 offen offset:2048 lds\n"
    "v_mfma_f32_16x16x32_bf16 v[48:51], a[128:131], a[152:155], v[48:51]\n"
    "s_waitcnt lgkmcnt(6)\n"
    "v_mfma_f32_16x16x32_bf16 v[52:55], a[108:111], a[144:147], v[52:55]\n"
    "ds_read_b64_tr_b16 a[120:121], v201 offset:40960\n"
    "ds_read_b128 a[196:199], v232 offset:8192\n"
    "v_mfma_f32_16x16x32_bf16 v[56:59], a[108:111], a[148:151], v[56:59]\n"
    "ds_read_b64_tr_b16 a[122:123], v201 offset:41984\n"
    "s_barrier\n"
    "v_mfma_f32_16x16x32_bf16 v[60:63], a[108:111], a[152:155], v[60:63]\n"
    "s_waitcnt lgkmcnt(6)\n"
    "v_mfma_f32_16x16x32_bf16 v[64:67], a[112:115], a[144:147], v[64:67]\n"
    "ds_read_b64_tr_b16 a[124:125], v194 offset:49152\n"
    "ds_read_b128 a[200:203], v232 offset:10240\n"
    "v_mfma_f32_16x16x32_bf16 v[68:71], a[112:115], a[148:151], v[68:71]\n"
    "ds_read_b64_tr_b16 a[126:127], v194 offset:50176\n"
    "v_mfma_f32_16x16x32_bf16 v[72:75], a[112:115], a[152:155], v[72:75]\n"
    "s_waitcnt lgkmcnt(6)\n"
    "v_mfma_f32_16x16x32_bf16 v[76:79], a[116:119], a[144:147], v[76:79]\n"
    "ds_read_b64_tr_b16 a[128:129], v195 offset:49152\n"
    "v_mfma_f32_16x16x32_bf16 v[80:83], a[116:119], a[148:151], v[80:83]\n"
    "ds_read_b64_tr_b16 a[130:131], v195 offset:50176\n"
    "v_mfma_f32_16x16x32_bf16 v[84:87], a[116:119], a[152:155], v[84:87]\n"
    "s_waitcnt lgkmcnt(5)\n"
    "v_mfma_f32_16x16x32_bf16 v[88:91], a[120:123], a[144:147], v[88:91]\n"
    "ds_read_b64_tr_b16 a[108:109], v196 offset:49152\n"
    "v_mfma_f32_16x16x32_bf16 v[92:95], a[120:123], a[148:151], v[92:95]\n"
    "ds_read_b64_tr_b16 a[110:111], v196 offset:50176\n"
    "v_mfma_f32_16x16x32_bf16 v[96:99], a[120:123], a[152:155], v[96:99]\n"
    "s_waitcnt lgkmcnt(4)\n"
    "v_mfma_f32_16x16x32_bf16 v[4:7], a[124:127], a[156:159], v[4:7]\n"
    "ds_read_b64_tr_b16 a[112:113], v197 offset:49152\n"
    "v_mfma_f32_16x16x32_bf16 v[8:11], a[124:127], a[160:163], v[8:11]\n"
    "ds_read_b64_tr_b16 a[114:115], v197 offset:50176\n"
    "v_mfma_f32_16x16x32_bf16 v[12:15], a[124:127], a[164:167], v[12:15]\n"
    "s_waitcnt lgkmcnt(4)\n"
    "v_mfma_f32_16x16x32_bf16 v[16:19], a[128:131], a[156:159], v[16:19]\n"
    "ds_read_b64_tr_b16 a[116:117], v198 offset:49152\n"
    "v_mfma_f32_16x16x32_bf16 v[20:23], a[128:131], a[160:163], v[20:23]\n"
    "ds_read_b64_tr_b16 a[118:119], v198 offset:50176\n"
    "v_mfma_f32_16x16x32_bf16 v[24:27], a[128:131], a[164:167], v[24:27]\n"
    "s_waitcnt lgkmcnt(4)\n"
    "v_mfma_f32_16x16x32_bf16 v[28:31], a[108:111], a[156:159], v[28:31]\n"
    "ds_read_b64_tr_b16 a[120:121], v199 offset:49152\n"
    "v_mfma_f32_16x16x32_bf16 v[32:35], a[108:111], a[160:163], v[32:35]\n"
    "ds_read_b64_tr_b16 a[122:123], v199 offset:50176\n"
    "v_mfma_f32_16x16x32_bf16 v[36:39], a[108:111], a[164:167], v[36:39]\n"
    "s_waitcnt lgkmcnt(4)\n"
    "v_mfma_f32_16x16x32_bf16 v[40:43], a[112:115], a[156:159], v[40:43]\n"
    "ds_read_b64_tr_b16 a[124:125], v200 offset:49152\n"
    "v_mfma_f32_16x16x32_bf16 v[44:47], a[112:115], a[160:163], v[44:47]\n"
    "ds_read_b64_tr_b16 a[126:127], v200 offset:50176\n"
    "v_mfma_f32_16x16x32_bf16 v[48:51], a[112:115], a[164:167], v[48:51]\n"
    "s_waitcnt lgkmcnt(4)\n"
    "v_mfma_f32_16x16x32_bf16 v[52:55], a[116:119], a[156:159], v[52:55]\n"
    "ds_read_b64_tr_b16 a[128:129], v201 offset:49152\n"
    "v_mfma_f32_16x16x32_bf16 v[56:59], a[116:119], a[160:163], v[56:59]\n"
    "ds_read_b64_tr_b16 a[130:131], v201 offset:50176\n"
    "v_mfma_f32_16x16x32_bf16 v[60:63], a[116:119], a[164:167], v[60:63]\n"
    "s_waitcnt lgkmcnt(4)\n"
    "v_mfma_f32_16x16x32_bf16 v[64:67], a[120:123], a[156:159], v[64:67]\n"
    "ds_read_b64_tr_b16 a[108:109], v194 offset:57344\n"
    "v_mfma_f32_16x16x32_bf16 v[68:71], a[120:123], a[160:163], v[68:71]\n"
    "ds_read_b64_tr_b16 a[110:111], v194 offset:58368\n"
    "v_mfma_f32_16x16x32_bf16 v[72:75], a[120:123], a[164:167], v[72:75]\n"
    "s_waitcnt lgkmcnt(4)\n"
    "v_mfma_f32_16x16x32_bf16 v[76:79], a[124:127], a[156:159], v[76:79]\n"
    "ds_read_b64_tr_b16 a[112:113], v195 offset:57344\n"
    "v_mfma_f32_16x16x32_bf16 v[80:83], a[124:127], a[160:163], v[80:83]\n"
    "ds_read_b64_tr_b16 a[114:115], v195 offset:58368\n"
    "v_mfma_f32_16x16x32_bf16 v[84:87], a[124:127], a[164:167], v[84:87]\n"
    "s_waitcnt lgkmcnt(4)\n"
    "v_mfma_f32_16x16x32_bf16 v[88:91], a[128:131], a[156:159], v[88:91]\n"
    "ds_read_b64_tr_b16 a[116:117], v196 offset:57344\n"
    "v_mfma_f32_16x16x32_bf16 v[92:95], a[128:131], a[160:163], v[92:95]\n"
    "ds_read_b64_tr_b16 a[118:119], v196 offset:58368\n"
    "v_mfma_f32_16x16x32_bf16 v[96:99], a[128:131], a[164:167], v[96:99]\n"
    "s_waitcnt lgkmcnt(4)\n"
    "v_mfma_f32_16x16x32_bf16 v[4:7], a[108:111], a[168:171], v[4:7]\n"
    "ds_read_b64_tr_b16 a[120:121], v197 offset:57344\n"
    "v_mfma_f32_16x16x32_bf16 v[8:11], a[108:111], a[172:175], v[8:11]\n"
    "ds_read_b64_tr_b16 a[122:123], v197 offset:58368\n"
    "v_mfma_f32_16x16x32_bf16 v[12:15], a[108:111], a[176:179], v[12:15]\n"
    "s_waitcnt lgkmcnt(4)\n"
    "v_mfma_f32_16x16x32_bf16 v[16:19], a[112:115], a[168:171], v[16:19]\n"
    "ds_read_b64_tr_b16 a[124:125], v198 offset:57344\n"
    "v_mfma_f32_16x16x32_bf16 v[20:23], a[112:115], a[172:175], v[20:23]\n"
    "ds_read_b64_tr_b16 a[126:127], v198 offset:58368\n"
    "v_mfma_f32_16x16x32_bf16 v[24:27], a[112:115], a[176:179], v[24:27]\n"
    "s_waitcnt lgkmcnt(4)\n"
    "v_mfma_f32_16x16x32_bf16 v[28:31], a[116:119], a[168:171], v[28:31]\n"
    "ds_read_b64_tr_b16 a[128:129], v199 offset:57344\n"
    "v_mfma_f32_16x16x32_bf16 v[32:35], a[116:119], a[172:175], v[32:35]\n"
    "ds_read_b64_tr_b16 a[130:131], v199 offset:58368\n"
    "v_mfma_f32_16x16x32_bf16 v[36:39], a[116:119], a[176:179], v[36:39]\n"
    "s_waitcnt lgkmcnt(4)\n"
    "v_mfma_f32_16x16x32_bf16 v[40:43], a[120:123], a[168:171], v[40:43]\n"
    "ds_read_b64_tr_b16 a[108:109], v200 offset:57344\n"
    "v_mfma_f32_16x16x32_bf16 v[44:47], a[120:123], a[172:175], v[44:47]\n"
    "ds_read_b64_tr_b16 a[110:111], v200 offset:58368\n"
    "v_mfma_f32_16x16x32_bf16 v[48:51], a[120:123], a[176:179], v[48:51]\n"
    "s_waitcnt lgkmcnt(4)\n"
    "v_mfma_f32_16x16x32_bf16 v[52:55], a[124:127], a[168:171], v[52:55]\n"
    "ds_read_b64_tr_b16 a[112:113], v201 offset:57344\n"
    "v_mfma_f32_16x16x32_bf16 v[56:59], a[124:127], a[172:175], v[56:59]\n"
    "ds_read_b64_tr_b16 a[114:115], v201 offset:58368\n"
    "s_waitcnt vmcnt(6)\n"
    "s_barrier\n"
    "v_mfma_f32_16x16x32_bf16 v[60:63], a[124:127], a[176:179], v[60:63]\n"
    "s_waitcnt lgkmcnt(4)\n"
    "s_add_u32 s64, s69, s47\n"
    "s_add_u32 m0, s50, 20480\n"
    "v_mfma_f32_16x16x32_bf16 v[64:67], a[128:131], a[168:171], v[64:67]\n"
    "ds_read_b64_tr_b16 a[116:117], v194 offset:0\n"
    "buffer_load_dwordx4 v226, s[24:27], s64 offen offset:0 lds\n"
    "v_mfma_f32_16x16x32_bf16 v[68:71], a[128:131], a[172:175], v[68:71]\n"
    "ds_read_b64_tr_b16 a[118:119], v194 offset:1024\n"
    "buffer_load_dwordx4 v227, s[24:27], s64 offen offset:1024 lds\n"
    "v_mfma_f32_16x16x32_bf16 v[72:75], a[128:131], a[176:179], v[72:75]\n"
    "s_waitcnt lgkmcnt(4)\n"
    "v_mfma_f32_16x16x32_bf16 v[76:79], a[108:111], a[168:171], v[76:79]\n"
    "ds_read_b64_tr_b16 a[120:121], v195 offset:0\n"
    "buffer_load_dwordx4 v228, s[24:27], s64 offen offset:2048 lds\n"
    "v_mfma_f32_16x16x32_bf16 v[80:83], a[108:111], a[172:175], v[80:83]\n"
    "s_add_u32 m0, s50, 23552\n"
    "ds_read_b64_tr_b16 a[122:123], v195 offset:1024\n"
    "v_mfma_f32_16x16x32_bf16 v[84:87], a[108:111], a[176:179], v[84:87]\n"
    "s_waitcnt lgkmcnt(4)\n"
    "buffer_load_dwordx4 v229, s[24:27], s64 offen offset:0 lds\n"
    "v_mfma_f32_16x16x32_bf16 v[88:91], a[112:115], a[168:171], v[88:91]\n"
    "ds_read_b64_tr_b16 a[124:125], v196 offset:0\n"
    "v_mfma_f32_16x16x32_bf16 v[92:95], a[112:115], a[172:175], v[92:95]\n"
    "ds_read_b64_tr_b16 a[126:127], v196 offset:1024\n"
    "buffer_load_dwordx4 v230, s[24:27], s64 offen offset:1024 lds\n"
    "v_mfma_f32_16x16x32_bf16 v[96:99], a[112:115], a[176:179], v[96:99]\n"
    "s_waitcnt lgkmcnt(4)\n"
    "v_mfma_f32_16x16x32_bf16 v[4:7], a[116:119], a[180:183], v[4:7]\n"
    "s_add_u32 s63, s65, s99\n"
    "ds_read_b64_tr_b16 a[128:129], v197 offset:0\n"
    "v_mfma_f32_16x16x32_bf16 v[8:11], a[116:119], a[184:187], v[8:11]\n"
    "s_add_u32 m0, s60, 0\n"
    "ds_read_b64_tr_b16 a[130:131], v197 offset:1024\n"
    "v_mfma_f32_16x16x32_bf16 v[12:15], a[116:119], a[188:191], v[12:15]\n"
    "s_waitcnt lgkmcnt(4)\n"
    "buffer_load_dwordx4 v220, s[16:19], s63 offen offset:0 lds\n"
    "v_mfma_f32_16x16x32_bf16 v[16:19], a[120:123], a[180:183], v[16:19]\n"
    "ds_read_b64_tr_b16 a[108:109], v198 offset:0\n"
    "buffer_load_dwordx4 v221, s[16:19], s63 offen offset:1024 lds\n"
    "v_mfma_f32_16x16x32_bf16 v[20:23], a[120:123], a[184:187], v[20:23]\n"
    "ds_read_b64_tr_b16 a[110:111], v198 offset:1024\n"
    "v_mfma_f32_16x16x32_bf16 v[24:27], a[120:123], a[188:191], v[24:27]\n"
    "s_waitcnt lgkmcnt(4)\n"
    "v_mfma_f32_16x16x32_bf16 v[28:31], a[124:127], a[180:183], v[28:31]\n"
    "ds_read_b64_tr_b16 a[112:113], v199 offset:0\n"
    "buffer_load_dwordx4 v222, s[16:19], s63 offen offset:2048 lds\n"
    "v_mfma_f32_16x16x32_bf16 v[32:35], a[124:127], a[184:187], v[32:35]\n"
    "s_add_u32 m0, s60, 3072\n"
    "ds_read_b64_tr_b16 a[114:115], v199 offset:1024\n"
    "v_mfma_f32_16x16x32_bf16 v[36:39], a[124:127], a[188:191], v[36:39]\n"
    "s_waitcnt lgkmcnt(4)\n"
    "v_mfma_f32_16x16x32_bf16 v[40:43], a[128:131], a[180:183], v[40:43]\n"
    "ds_read_b64_tr_b16 a[116:117], v200 offset:0\n"
    "buffer_load_dwordx4 v223, s[16:19], s63 offen offset:0 lds\n"
    "v_mfma_f32_16x16x32_bf16 v[44:47], a[128:131], a[184:187], v[44:47]\n"
    "ds_read_b64_tr_b16 a[118:119], v200 offset:1024\n"
    "v_mfma_f32_16x16x32_bf16 v[48:51], a[128:131], a[188:191], v[48:51]\n"
    "s_waitcnt lgkmcnt(4)\n"
    "v_mfma_f32_16x16x32_bf16 v[52:55], a[108:111], a[180:183], v[52:55]\n"
    "ds_read_b64_tr_b16 a[120:121], v201 offset:0\n"
    "buffer_load_dwordx4 v224, s[16:19], s63 offen offset:1024 lds\n"
    "v_mfma_f32_16x16x32_bf16 v[56:59], a[108:111], a[184:187], v[56:59]\n"
    "ds_read_b64_tr_b16 a[122:123], v201 offset:1024\n"
    "v_mfma_f32_16x16x32_bf16 v[60:63], a[108:111], a[188:191], v[60:63]\n"
    "s_waitcnt lgkmcnt(4)\n"
    "v_mfma_f32_16x16x32_bf16 v[64:67], a[112:115], a[180:183], v[64:67]\n"
    "ds_read_b64_tr_b16 a[124:125], v194 offset:8192\n"
    "buffer_load_dwordx4 v225, s[16:19], s63 offen offset:2048 lds\n"
    "v_mfma_f32_16x16x32_bf16 v[68:71], a[112:115], a[184:187], v[68:71]\n"
    "ds_read_b64_tr_b16 a[126:127], v194 offset:9216\n"
    "v_mfma_f32_16x16x32_bf16 v[72:75], a[112:115], a[188:191], v[72:75]\n"
    "s_waitcnt lgkmcnt(4)\n"
    "v_mfma_f32_16x16x32_bf16 v[76:79], a[116:119], a[180:183], v[76:79]\n"
    "ds_read_b64_tr_b16 a[128:129], v195 offset:8192\n"
    "v_mfma_f32_16x16x32_bf16 v[80:83], a[116:119], a[184:187], v[80:83]\n"
    "ds_read_b64_tr_b16 a[130:131], v195 offset:9216\n"
    "v_mfma_f32_16x16x32_bf16 v[84:87], a[116:119], a[188:191], v[84:87]\n"
    "s_waitcnt lgkmcnt(4)\n"
    "v_mfma_f32_16x16x32_bf16 v[88:91], a[120:123], a[180:183], v[88:91]\n"
    "ds_read_b64_tr_b16 a[108:109], v196 offset:8192\n"
    "v_mfma_f32_16x16x32_bf16 v[92:95], a[120:123], a[184:187], v[92:95]\n"
    "ds_read_b64_tr_b16 a[110:111], v196 offset:9216\n"
    "v_mfma_f32_16x16x32_bf16 v[96:99], a[120:123], a[188:191], v[96:99]\n"
    "s_waitcnt lgkmcnt(4)\n"
    "v_mfma_f32_16x16x32_bf16 v[4:7], a[124:127], a[192:195], v[4:7]\n"
    "ds_read_b64_tr_b16 a[112:113], v197 offset:8192\n"
    "v_mfma_f32_16x16x32_bf16 v[8:11], a[124:127], a[196:199], v[8:11]\n"
    "ds_read_b64_tr_b16 a[114:115], v197 offset:9216\n"
    "v_mfma_f32_16x16x32_bf16 v[12:15], a[124:127], a[200:203], v[12:15]\n"
    "s_waitcnt lgkmcnt(4)\n"
    "v_mfma_f32_16x16x32_bf16 v[16:19], a[128:131], a[192:195], v[16:19]\n"
    "ds_read_b64_tr_b16 a[116:117], v198 offset:8192\n"
    "v_mfma_f32_16x16x32_bf16 v[20:23], a[128:131], a[196:199], v[20:23]\n"
    "ds_read_b64_tr_b16 a[118:119], v198 offset:9216\n"
    "v_mfma_f32_16x16x32_bf16 v[24:27], a[128:131], a[200:203], v[24:27]\n"
    "s_waitcnt lgkmcnt(4)\n"
    "v_mfma_f32_16x16x32_bf16 v[28:31], a[108:111], a[192:195], v[28:31]\n"
    "ds_read_b64_tr_b16 a[120:121], v199 offset:8192\n"
    "v_mfma_f32_16x16x32_bf16 v[32:35], a[108:111], a[196:199], v[32:35]\n"
    "ds_read_b64_tr_b16 a[122:123], v199 offset:9216\n"
    "v_cvt_pk_bf16_f32 v112, v4, v5\n"
    "v_mfma_f32_16x16x32_bf16 v[36:39], a[108:111], a[200:203], v[36:39]\n"
    "s_waitcnt lgkmcnt(4)\n"
    "v_cvt_pk_bf16_f32 v113, v6, v7\n"
    "v_cvt_pk_bf16_f32 v114, v16, v17\n"
    "v_mfma_f32_16x16x32_bf16 v[40:43], a[112:115], a[192:195], v[40:43]\n"
    "ds_read_b64_tr_b16 a[124:125], v200 offset:8192\n"
    "v_cvt_pk_bf16_f32 v115, v18, v19\n"
    "v_mfma_f32_16x16x32_bf16 v[44:47], a[112:115], a[196:199], v[44:47]\n"
    "ds_read_b64_tr_b16 a[126:127], v200 offset:9216\n"
    "v_cvt_pk_bf16_f32 v116, v8, v9\n"
    "v_mfma_f32_16x16x32_bf16 v[48:51], a[112:115], a[200:203], v[48:51]\n"
    "s_waitcnt lgkmcnt(4)\n"
    "v_cvt_pk_bf16_f32 v117, v10, v11\n"
    "v_cvt_pk_bf16_f32 v118, v20, v21\n"
    "v_mfma_f32_16x16x32_bf16 v[52:55], a[116:119], a[192:195], v[52:55]\n"
    "ds_read_b64_tr_b16 a[128:129], v201 offset:8192\n"
    "v_cvt_pk_bf16_f32 v119, v22, v23\n"
    "v_mfma_f32_16x16x32_bf16 v[56:59], a[116:119], a[196:199], v[56:59]\n"
    "ds_read_b64_tr_b16 a[130:131], v201 offset:9216\n"
    "s_barrier\n"
    "v_cvt_pk_bf16_f32 v120, v12, v13\n"
    "v_mfma_f32_16x16x32_bf16 v[60:63], a[116:119], a[200:203], v[60:63]\n"
    "s_waitcnt lgkmcnt(4)\n"
    "v_cvt_pk_bf16_f32 v121, v14, v15\n"
    "v_cvt_pk_bf16_f32 v122, v24, v25\n"
    "v_mfma_f32_16x16x32_bf16 v[64:67], a[120:123], a[192:195], v[64:67]\n"
    "v_cvt_pk_bf16_f32 v123, v26, v27\n"
    "v_cvt_pk_bf16_f32 v124, v28, v29\n"
    "v_mfma_f32_16x16x32_bf16 v[68:71], a[120:123], a[196:199], v[68:71]\n"
    "v_cvt_pk_bf16_f32 v125, v30, v31\n"
    "v_cvt_pk_bf16_f32 v126, v40, v41\n"
    "v_mfma_f32_16x16x32_bf16 v[72:75], a[120:123], a[200:203], v[72:75]\n"
    "s_waitcnt lgkmcnt(2)\n"
    "v_cvt_pk_bf16_f32 v127, v42, v43\n"
    "v_cvt_pk_bf16_f32 v128, v32, v33\n"
    "v_mfma_f32_16x16x32_bf16 v[76:79], a[124:127], a[192:195], v[76:79]\n"
    "v_cvt_pk_bf16_f32 v129, v34, v35\n"
    "v_cvt_pk_bf16_f32 v130, v44, v45\n"
    "v_mfma_f32_16x16x32_bf16 v[80:83], a[124:127], a[196:199], v[80:83]\n"
    "v_cvt_pk_bf16_f32 v131, v46, v47\n"
    "v_cvt_pk_bf16_f32 v132, v36, v37\n"
    "v_mfma_f32_16x16x32_bf16 v[84:87], a[124:127], a[200:203], v[84:87]\n"
    "s_waitcnt lgkmcnt(0)\n"
    "v_cvt_pk_bf16_f32 v133, v38, v39\n"
    "v_cvt_pk_bf16_f32 v134, v48, v49\n"
    "v_mfma_f32_16x16x32_bf16 v[88:91], a[128:131], a[192:195], v[88:91]\n"
    "v_cvt_pk_bf16_f32 v135, v50, v51\n"
    "v_cvt_pk_bf16_f32 v136, v52, v53\n"
    "v_mfma_f32_16x16x32_bf16 v[92:95], a[128:131], a[196:199], v[92:95]\n"
    "v_cvt_pk_bf16_f32 v137, v54, v55\n"
    "v_cvt_pk_bf16_f32 v138, v64, v65\n"
    "v_mfma_f32_16x16x32_bf16 v[96:99], a[128:131], a[200:203], v[96:99]\n"
    "v_cvt_pk_bf16_f32 v139, v66, v67\n"
    "v_cvt_pk_bf16_f32 v140, v56, v57\n"
    "v_mfma_f32_16x16x32_bf16 v[160:163], a[204:207], v[112:115], 0\n"
    "v_cvt_pk_bf16_f32 v141, v58, v59\n"
    "v_cvt_pk_bf16_f32 v142, v68, v69\n"
    "v_mfma_f32_16x16x32_bf16 v[164:167], a[208:211], v[112:115], 0\n"
    "v_cvt_pk_bf16_f32 v143, v70, v71\n"
    "v_cvt_pk_bf16_f32 v144, v60, v61\n"
    "v_mfma_f32_16x16x32_bf16 v[168:171], a[212:215], v[112:115], 0\n"
    "v_cvt_pk_bf16_f32 v145, v62, v63\n"
    "v_cvt_pk_bf16_f32 v146, v72, v73\n"
    "v_mfma_f32_16x16x32_bf16 v[172:175], a[204:207], v[116:119], 0\n"
    "v_cvt_pk_bf16_f32 v147, v74, v75\n"
    "v_cvt_pk_bf16_f32 v148, v76, v77\n"
    "v_mfma_f32_16x16x32_bf16 v[176:179], a[208:211], v[116:119], 0\n"
    "v_cvt_pk_bf16_f32 v149, v78, v79\n"
    "v_cvt_pk_bf16_f32 v150, v88, v89\n"
    "v_mfma_f32_16x16x32_bf16 v[180:183], a[212:215], v[116:119], 0\n"
    "v_cvt_pk_bf16_f32 v151, v90, v91\n"
    "v_cvt_pk_bf16_f32 v152, v80, v81\n"
    "v_mfma_f32_16x16x32_bf16 v[192:195], a[204:207], v[120:123], 0\n"
    "v_cvt_pk_bf16_f32 v153, v82, v83\n"
    "v_cvt_pk_bf16_f32 v154, v92, v93\n"
    "v_mfma_f32_16x16x32_bf16 v[204:207], a[208:211], v[120:123], 0\n"
    "v_cvt_pk_bf16_f32 v155, v94, v95\n"
    "v_cvt_pk_bf16_f32 v156, v84, v85\n"
    "v_mfma_f32_16x16x32_bf16 v[216:219], a[212:215], v[120:123], 0\n"
    "v_cvt_pk_bf16_f32 v157, v86, v87\n"
    "v_cvt_pk_bf16_f32 v158, v96, v97\n"
    "v_mfma_f32_16x16x32_bf16 v[160:163], a[216:219], v[124:127], v[160:163]\n"
    "v_cvt_pk_bf16_f32 v159, v98, v99\n"
    "v_mfma_f32_16x16x32_bf16 v[164:167], a[220:223], v[124:127], v[164:167]\n"
    "v_mfma_f32_16x16x32_bf16 v[168:171], a[224:227], v[124:127], v[168:171]\n"
    "v_mfma_f32_16x16x32_bf16 v[172:175], a[216:219], v[128:131], v[172:175]\n"
    "s_waitcnt vmcnt(11)\n"
    "ds_read_b128 a[132:135], v231 offset:12288\n"
    "L_join_%=:\n"
    "v_mfma_f32_16x16x32_bf16 v[176:179], a[220:223], v[128:131], v[176:179]\n"
    "v_mfma_f32_16x16x32_bf16 v[180:183], a[224:227], v[128:131], v[180:183]\n"
    "ds_read_b128 a[136:139], v231 offset:14336\n"
    "v_mfma_f32_16x16x32_bf16 v[192:195], a[216:219], v[132:135], v[192:195]\n"
    "v_mfma_f32_16x16x32_bf16 v[204:207], a[220:223], v[132:135], v[204:207]\n"
    "ds_read_b128 a[140:143], v231 offset:16384\n"
    "v_mfma_f32_16x16x32_bf16 v[216:219], a[224:227], v[132:135], v[216:219]\n"
    "v_mfma_f32_16x16x32_bf16 v[160:163], a[228:231], v[136:139], v[160:163]\n"
    "ds_read_b128 a[144:147], v232 offset:12288\n"
    "v_mfma_f32_16x16x32_bf16 v[164:167], a[232:235], v[136:139], v[164:167]\n"
    "v_mfma_f32_16x16x32_bf16 v[168:171], a[236:239], v[136:139], v[168:171]\n"
    "ds_read_b128 a[148:151], v232 offset:14336\n"
    "v_mfma_f32_16x16x32_bf16 v[172:175], a[228:231], v[140:143], v[172:175]\n"
    "v_mfma_f32_16x16x32_bf16 v[176:179], a[232:235], v[140:143], v[176:179]\n"
    "s_barrier\n"
    "ds_read_b128 a[152:155], v232 offset:16384\n"
    "v_mfma_f32_16x16x32_bf16 v[180:183], a[236:239], v[140:143], v[180:183]\n"
    "s_add_u32 s64, s70, s46\n"
    "s_add_u32 m0, s50, 40960\n"
    "s_nop 0\n"
    "buffer_load_dwordx4 v226, s[24:27], s64 offen offset:0 lds\n"
    "v_mfma_f32_16x16x32_bf16 v[192:195], a[228:231], v[144:147], v[192:195]\n"
    "ds_read_b64_tr_b16 a[108:109], v235 offset:0\n"
    "v_mfma_f32_16x16x32_bf16 v[204:207], a[232:235], v[144:147], v[204:207]\n"
    "ds_read_b64_tr_b16 a[110:111], v235 offset:1280\n"
    "v_mfma_f32_16x16x32_bf16 v[216:219], a[236:239], v[144:147], v[216:219]\n"
    "buffer_load_dwordx4 v227, s[24:27], s64 offen offset:1024 lds\n"
    "v_mfma_f32_16x16x32_bf16 v[160:163], a[240:243], v[148:151], v[160:163]\n"
    "ds_read_b64_tr_b16 a[112:113], v236 offset:32\n"
    "v_mfma_f32_16x16x32_bf16 v[164:167], a[244:247], v[148:151], v[164:167]\n"
    "ds_read_b64_tr_b16 a[114:115], v236 offset:1312\n"
    "v_mfma_f32_16x16x32_bf16 v[168:171], a[248:251], v[148:151], v[168:171]\n"
    "buffer_load_dwordx4 v228, s[24:27], s64 offen offset:2048 lds\n"
    "v_mfma_f32_16x16x32_bf16 v[172:175], a[240:243], v[152:155], v[172:175]\n"
    "ds_read_b64_tr_b16 a[116:117], v235 offset:64\n"
    "v_mfma_f32_16x16x32_bf16 v[176:179], a[244:247], v[152:155], v[176:179]\n"
    "s_add_u32 m0, s50, 44032\n"
    "ds_read_b64_tr_b16 a[118:119], v235 offset:1344\n"
    "v_mfma_f32_16x16x32_bf16 v[180:183], a[248:251], v[152:155], v[180:183]\n"
    "v_mfma_f32_16x16x32_bf16 v[192:195], a[240:243], v[156:159], v[192:195]\n"
    "buffer_load_dwordx4 v229, s[24:27], s64 offen offset:0 lds\n"
    "v_mfma_f32_16x16x32_bf16 v[204:207], a[244:247], v[156:159], v[204:207]\n"
    "v_mfma_f32_16x16x32_bf16 v[216:219], a[248:251], v[156:159], v[216:219]\n"
    "s_waitcnt lgkmcnt(4)\n"
    "v_mfma_f32_16x16x32_bf16 v[4:7], a[132:135], a[108:111], 0\n"
    "ds_read_b64_tr_b16 a[120:121], v236 offset:96\n"
    "buffer_load_dwordx4 v230, s[24:27], s64 offen offset:1024 lds\n"
    "v_mfma_f32_16x16x32_bf16 v[8:11], a[136:139], a[108:111], 0\n"
    "ds_read_b64_tr_b16 a[122:123], v236 offset:1376\n"
    "v_max3_f32 v250, v160, v161, v162\n"
    "v_max3_f32 v254, v172, v173, v174\n"
    "v_mfma_f32_16x16x32_bf16 v[12:15], a[140:143], a[108:111], 0\n"
    "s_waitcnt lgkmcnt(4)\n"
    "v_max3_f32 v251, v163, v164, v165\n"
    "v_max3_f32 v255, v175, v176, v177\n"
    "v_max3_f32 v252, v166, v167, v168\n"
    "v_mfma_f32_16x16x32_bf16 v[16:19], a[132:135], a[112:115], 0\n"
    "ds_read_b64_tr_b16 a[124:125], v235 offset:128\n"
    "v_max3_f32 v1, v178, v179, v180\n"
    "v_max3_f32 v253, v169, v170, v171\n"
    "v_mfma_f32_16x16x32_bf16 v[20:23], a[136:139], a[112:115], 0\n"
    "s_add_u32 s63, s66, s98\n"
    "ds_read_b64_tr_b16 a[126:127], v235 offset:1408\n"
    "v_max3_f32 v2, v181, v182, v183\n"
    "v_mfma_f32_16x16x32_bf16 v[24:27], a[140:143], a[112:115], 0\n"
    "s_waitcnt lgkmcnt(4)\n"
    "v_max3_f32 v250, v250, v251, v252\n"
    "v_max3_f32 v254, v254, v255, v1\n"
    "v_max_f32 v250, v250, v253\n"
    "v_mfma_f32_16x16x32_bf16 v[28:31], a[132:135], a[116:119], 0\n"
    "s_add_u32 m0, s60, 6144\n"
    "ds_read_b64_tr_b16 a[128:129], v236 offset:160\n"
    "buffer_load_dwordx4 v220, s[16:19], s63 offen offset:0 lds\n"
    "v_max_f32 v254, v254, v2\n"
    "v_mov_b32 v251, v250\n"
    "v_mfma_f32_16x16x32_bf16 v[32:35], a[136:139], a[116:119], 0\n"
    "ds_read_b64_tr_b16 a[130:131], v236 offset:1440\n"
    "buffer_load_dwordx4 v221, s[16:19], s63 offen offset:1024 lds\n"
    "v_mov_b32 v255, v254\n"
    "v_mfma_f32_16x16x32_bf16 v[36:39], a[140:143], a[116:119], 0\n"
    "s_waitcnt lgkmcnt(4)\n"
    "v_permlane32_swap_b32 v250, v251\n"
    "v_mfma_f32_16x16x32_bf16 v[40:43], a[132:135], a[120:123], 0\n"
    "ds_read_b64_tr_b16 a[108:109], v235 offset:192\n"
    "v_permlane32_swap_b32 v254, v255\n"
    "v_max_f32 v250, v250, v251\n"
    "v_max_f32 v254, v254, v255\n"
    "v_mfma_f32_16x16x32_bf16 v[44:47], a[136:139], a[120:123], 0\n"
    "ds_read_b64_tr_b16 a[110:111], v235 offset:1472\n"
    "v_mov_b32 v251, v250\n"
    "v_mov_b32 v255, v254\n"
    "v_mfma_f32_16x16x32_bf16 v[48:51], a[140:143], a[120:123], 0\n"
    "s_waitcnt lgkmcnt(4)\n"
    "buffer_load_dwordx4 v222, s[16:19], s63 offen offset:2048 lds\n"
    "v_permlane16_swap_b32 v250, v251\n"
    "v_mfma_f32_16x16x32_bf16 v[52:55], a[132:135], a[124:127], 0\n"
    "s_add_u32 m0, s60, 9216\n"
    "ds_read_b64_tr_b16 a[112:113], v236 offset:224\n"
    "v_permlane16_swap_b32 v254, v255\n"
    "v_mfma_f32_16x16x32_bf16 v[56:59], a[136:139], a[124:127], 0\n"
    "ds_read_b64_tr_b16 a[114:115], v236 offset:1504\n"
    "v_max_f32 v250, v250, v251\n"
    "v_max_f32 v254, v254, v255\n"
    "v_mul_f32 v250, s80, v250\n"
    "v_mfma_f32_16x16x32_bf16 v[60:63], a[140:143], a[124:127], 0\n"
    "s_waitcnt lgkmcnt(4)\n"
    "buffer_load_dwordx4 v223, s[16:19], s63 offen offset:0 lds\n"
    "v_mul_f32 v254, s80, v254\n"
    "v_fma_f32 v160, v160, s80, -v250\n"
    "v_mfma_f32_16x16x32_bf16 v[64:67], a[132:135], a[128:131], 0\n"
    "ds_read_b64_tr_b16 a[116:117], v235 offset:256\n"
    "v_fma_f32 v172, v172, s80, -v254\n"
    "v_fma_f32 v161, v161, s80, -v250\n"
    "v_mfma_f32_16x16x32_bf16 v[68:71], a[136:139], a[128:131], 0\n"
    "ds_read_b64_tr_b16 a[118:119], v235 offset:1536\n"
    "v_fma_f32 v173, v173, s80, -v254\n"
    "v_mfma_f32_16x16x32_bf16 v[72:75], a[140:143], a[128:131], 0\n"
    "s_waitcnt lgkmcnt(4)\n"
    "v_exp_f32 v160, v160\n"
    "v_exp_f32 v172, v172\n"
    "v_fma_f32 v162, v162, s80, -v250\n"
    "v_mfma_f32_16x16x32_bf16 v[76:79], a[132:135], a[108:111], 0\n"
    "ds_read_b64_tr_b16 a[120:121], v235 offset:10240\n"
    "buffer_load_dwordx4 v224, s[16:19], s63 offen offset:1024 lds\n"
    "v_fma_f32 v174, v174, s80, -v254\n"
    "v_mfma_f32_16x16x32_bf16 v[80:83], a[136:139], a[108:111], 0\n"
    "ds_read_b64_tr_b16 a[122:123], v235 offset:11520\n"
    "v_exp_f32 v161, v161\n"
    "v_mfma_f32_16x16x32_bf16 v[84:87], a[140:143], a[108:111], 0\n"
    "s_waitcnt lgkmcnt(4)\n"
    "v_exp_f32 v173, v173\n"
    "v_fma_f32 v163, v163, s80, -v250\n"
    "v_mfma_f32_16x16x32_bf16 v[88:91], a[132:135], a[112:115], 0\n"
    "ds_read_b64_tr_b16 a[124:125], v236 offset:10272\n"
    "buffer_load_dwordx4 v225, s[16:19], s63 offen offset:2048 lds\n"
    "v_fma_f32 v175, v175, s80, -v254\n"
    "v_mfma_f32_16x16x32_bf16 v[92:95], a[136:139], a[112:115], 0\n"
    "ds_read_b64_tr_b16 a[126:127], v236 offset:11552\n"
    "v_exp_f32 v162, v162\n"
    "v_mfma_f32_16x16x32_bf16 v[96:99], a[140:143], a[112:115], 0\n"
    "s_waitcnt lgkmcnt(4)\n"
    "v_exp_f32 v174, v174\n"
    "v_fma_f32 v164, v164, s80, -v250\n"
    "v_fma_f32 v176, v176, s80, -v254\n"
    "v_mfma_f32_16x16x32_bf16 v[100:103], a[132:135], a[116:119], 0\n"
    "ds_read_b64_tr_b16 a[128:129], v235 offset:10304\n"
    "v_exp_f32 v163, v163\n"
    "v_mfma_f32_16x16x32_bf16 v[104:107], a[136:139], a[116:119], 0\n"
    "ds_read_b64_tr_b16 a[130:131], v235 offset:11584\n"
    "v_exp_f32 v175, v175\n"
    "v_mfma_f32_16x16x32_bf16 v[108:111], a[140:143], a[116:119], 0\n"
    "s_waitcnt lgkmcnt(4)\n"
    "v_fma_f32 v165, v165, s80, -v250\n"
    "v_fma_f32 v177, v177, s80, -v254\n"
    "v_exp_f32 v164, v164\n"
    "v_mfma_f32_16x16x32_bf16 v[4:7], a[144:147], a[120:123], v[4:7]\n"
    "s_waitcnt vmcnt(11)\n"
    "ds_read_b64_tr_b16 a[108:109], v236 offset:10336\n"
    "v_mfma_f32_16x16x32_bf16 v[8:11], a[148:151], a[120:123], v[8:11]\n"
    "ds_read_b64_tr_b16 a[110:111], v236 offset:11616\n"
    "ds_read_b128 a[156:159], v231 offset:0\n"
    "v_exp_f32 v176, v176\n"
    "v_mfma_f32_16x16x32_bf16 v[12:15], a[152:155], a[120:123], v[12:15]\n"
    "s_waitcnt lgkmcnt(5)\n"
    "ds_read_b128 a[160:163], v231 offset:2048\n"
    "v_fma_f32 v166, v166, s80, -v250\n"
    "v_fma_f32 v178, v178, s80, -v254\n"
    "v_mfma_f32_16x16x32_bf16 v[16:19], a[144:147], a[124:127], v[16:19]\n"
    "ds_read_b64_tr_b16 a[112:113], v235 offset:10368\n"
    "v_exp_f32 v165, v165\n"
    "v_mfma_f32_16x16x32_bf16 v[20:23], a[148:151], a[124:127], v[20:23]\n"
    "ds_read_b64_tr_b16 a[114:115], v235 offset:11648\n"
    "ds_read_b128 a[164:167], v231 offset:4096\n"
    "v_exp_f32 v177, v177\n"
    "v_mfma_f32_16x16x32_bf16 v[24:27], a[152:155], a[124:127], v[24:27]\n"
    "s_waitcnt lgkmcnt(7)\n"
    "v_fma_f32 v167, v167, s80, -v250\n"
    "v_fma_f32 v179, v179, s80, -v254\n"
    "v_mfma_f32_16x16x32_bf16 v[28:31], a[144:147], a[128:131], v[28:31]\n"
    "ds_read_b64_tr_b16 a[116:117], v236 offset:10400\n"
    "ds_read_b128 a[168:171], v232 offset:0\n"
    "v_exp_f32 v166, v166\n"
    "v_mfma_f32_16x16x32_bf16 v[32:35], a[148:151], a[128:131], v[32:35]\n"
    "ds_read_b64_tr_b16 a[118:119], v236 offset:11680\n"
    "v_exp_f32 v178, v178\n"
    "v_fma_f32 v168, v168, s80, -v250\n"
    "v_mfma_f32_16x16x32_bf16 v[36:39], a[152:155], a[128:131], v[36:39]\n"
    "s_waitcnt lgkmcnt(8)\n"
    "ds_read_b128 a[172:175], v232 offset:2048\n"
    "v_fma_f32 v180, v180, s80, -v254\n"
    "v_mfma_f32_16x16x32_bf16 v[40:43], a[144:147], a[108:111], v[40:43]\n"
    "ds_read_b64_tr_b16 a[120:121], v235 offset:10432\n"
    "v_exp_f32 v167, v167\n"
    "v_mfma_f32_16x16x32_bf16 v[44:47], a[148:151], a[108:111], v[44:47]\n"
    "ds_read_b64_tr_b16 a[122:123], v235 offset:11712\n"
    "ds_read_b128 a[176:179], v232 offset:4096\n"
    "v_exp_f32 v179, v179\n"
    "v_mfma_f32_16x16x32_bf16 v[48:51], a[152:155], a[108:111], v[48:51]\n"
    "s_waitcnt lgkmcnt(8)\n"
    "v_fma_f32 v169, v169, s80, -v250\n"
    "v_fma_f32 v181, v181, s80, -v254\n"
    "v_exp_f32 v168, v168\n"
    "v_mfma_f32_16x16x32_bf16 v[52:55], a[144:147], a[112:115], v[52:55]\n"
    "ds_read_b64_tr_b16 a[124:125], v236 offset:10464\n"
    "v_exp_f32 v180, v180\n"
    "v_mfma_f32_16x16x32_bf16 v[56:59], a[148:151], a[112:115], v[56:59]\n"
    "ds_read_b64_tr_b16 a[126:127], v236 offset:11744\n"
    "v_fma_f32 v170, v170, s80, -v250\n"
    "v_fma_f32 v182, v182, s80, -v254\n"
    "v_mfma_f32_16x16x32_bf16 v[60:63], a[152:155], a[112:115], v[60:63]\n"
    "s_waitcnt lgkmcnt(6)\n"
    "v_exp_f32 v169, v169\n"
    "v_mfma_f32_16x16x32_bf16 v[64:67], a[144:147], a[116:119], v[64:67]\n"
    "ds_read_b64_tr_b16 a[128:129], v235 offset:10496\n"
    "v_exp_f32 v181, v181\n"
    "v_fma_f32 v171, v171, s80, -v250\n"
    "v_fma_f32 v183, v183, s80, -v254\n"
    "v_mfma_f32_16x16x32_bf16 v[68:71], a[148:151], a[116:119], v[68:71]\n"
    "ds_read_b64_tr_b16 a[130:131], v235 offset:11776\n"
    "s_barrier\n"
    "v_mfma_f32_16x16x32_bf16 v[72:75], a[152:155], a[116:119], v[72:75]\n"
    "s_waitcnt lgkmcnt(5)\n"
    "v_exp_f32 v170, v170\n"
    "v_exp_f32 v182, v182\n"
    "v_mfma_f32_16x16x32_bf16 v[76:79], a[144:147], a[120:123], v[76:79]\n"
    "ds_read_b64_tr_b16 a[108:109], v233 offset:20480\n"
    "v_exp_f32 v171, v171\n"
    "v_mfma_f32_16x16x32_bf16 v[80:83], a[148:151], a[120:123], v[80:83]\n"
    "ds_read_b64_tr_b16 a[110:111], v233 offset:21760\n"
    "v_exp_f32 v183, v183\n"
    "v_mfma_f32_16x16x32_bf16 v[84:87], a[152:155], a[120:123], v[84:87]\n"
    "s_waitcnt lgkmcnt(4)\n"
    "v_add_f32 v251, v160, v161\n"
    "v_add_f32 v255, v172, v173\n"
    "v_add_f32 v252, v162, v163\n"
    "v_add_f32 v1, v174, v175\n"
    "v_mfma_f32_16x16x32_bf16 v[88:91], a[144:147], a[124:127], v[88:91]\n"
    "ds_read_b64_tr_b16 a[112:113], v234 offset:20512\n"
    "v_add_f32 v253, v164, v165\n"
    "v_add_f32 v2, v176, v177\n"
    "v_mfma_f32_16x16x32_bf16 v[92:95], a[148:151], a[124:127], v[92:95]\n"
    "ds_read_b64_tr_b16 a[114:115], v234 offset:21792\n"
    "v_add_f32 v251, v251, v166\n"
    "v_add_f32 v255, v255, v178\n"
    "v_mfma_f32_16x16x32_bf16 v[96:99], a[152:155], a[124:127], v[96:99]\n"
    "s_waitcnt lgkmcnt(4)\n"
    "v_add_f32 v252, v252, v167\n"
    "v_add_f32 v1, v1, v179\n"
    "v_add_f32 v253, v253, v168\n"
    "v_mfma_f32_16x16x32_bf16 v[100:103], a[144:147], a[128:131], v[100:103]\n"
    "ds_read_b64_tr_b16 a[116:117], v233 offset:20544\n"
    "v_add_f32 v2, v2, v180\n"
    "v_add_f32 v251, v251, v169\n"
    "v_add_f32 v255, v255, v181\n"
    "v_mfma_f32_16x16x32_bf16 v[104:107], a[148:151], a[128:131], v[104:107]\n"
    "ds_read_b64_tr_b16 a[118:119], v233 offset:21824\n"
    "v_add_f32 v252, v252, v170\n"
    "v_add_f32 v1, v1, v182\n"
    "v_mfma_f32_16x16x32_bf16 v[108:111], a[152:155], a[128:131], v[108:111]\n"
    "s_waitcnt lgkmcnt(4)\n"
    "v_add_f32 v253, v253, v171\n"
    "v_add_f32 v2, v2, v183\n"
    "v_add_f32 v252, v251, v252\n"
    "v_mfma_f32_16x16x32_bf16 v[4:7], a[156:159], a[108:111], v[4:7]\n"
    "ds_read_b64_tr_b16 a[120:121], v234 offset:20576\n"
    "v_add_f32 v1, v255, v1\n"
    "v_mfma_f32_16x16x32_bf16 v[8:11], a[160:163], a[108:111], v[8:11]\n"
    "ds_read_b64_tr_b16 a[122:123], v234 offset:21856\n"
    "v_add_f32 v252, v252, v253\n"
    "v_add_f32 v1, v1, v2\n"
    "v_mfma_f32_16x16x32_bf16 v[12:15], a[164:167], a[108:111], v[12:15]\n"
    "s_waitcnt lgkmcnt(4)\n"
    "v_mov_b32 v251, v252\n"
    "v_mov_b32 v255, v1\n"
    "v_mfma_f32_16x16x32_bf16 v[16:19], a[156:159], a[112:115], v[16:19]\n"
    "s_add_u32 s64, s70, s47\n"
    "s_add_u32 m0, s50, 0\n"
    "ds_read_b64_tr_b16 a[124:125], v233 offset:20608\n"
    "v_mfma_f32_16x16x32_bf16 v[20:23], a[160:163], a[112:115], v[20:23]\n"
    "ds_read_b64_tr_b16 a[126:127], v233 offset:21888\n"
    "buffer_load_dwordx4 v226, s[24:27], s64 offen offset:0 lds\n"
    "v_permlane32_swap_b32 v252, v251\n"
    "v_mfma_f32_16x16x32_bf16 v[24:27], a[164:167], a[112:115], v[24:27]\n"
    "s_waitcnt lgkmcnt(4)\n"
    "buffer_load_dwordx4 v227, s[24:27], s64 offen offset:1024 lds\n"
    "v_permlane32_swap_b32 v1, v255\n"
    "v_add_f32 v252, v252, v251\n"
    "v_add_f32 v1, v1, v255\n"
    "v_mov_b32 v251, v252\n"
    "v_mfma_f32_16x16x32_bf16 v[28:31], a[156:159], a[116:119], v[28:31]\n"
    "ds_read_b64_tr_b16 a[128:129], v234 offset:20640\n"
    "buffer_load_dwordx4 v228, s[24:27], s64 offen offset:2048 lds\n"
    "v_mov_b32 v255, v1\n"
    "v_mfma_f32_16x16x32_bf16 v[32:35], a[160:163], a[116:119], v[32:35]\n"
    "ds_read_b64_tr_b16 a[130:131], v234 offset:21920\n"
    "v_permlane16_swap_b32 v252, v251\n"
    "v_mfma_f32_16x16x32_bf16 v[36:39], a[164:167], a[116:119], v[36:39]\n"
    "s_waitcnt lgkmcnt(4)\n"
    "v_permlane16_swap_b32 v1, v255\n"
    "v_mfma_f32_16x16x32_bf16 v[40:43], a[156:159], a[120:123], v[40:43]\n"
    "s_add_u32 m0, s50, 3072\n"
    "ds_read_b64_tr_b16 a[108:109], v233 offset:20672\n"
    "v_add_f32 v252, v252, v251\n"
    "v_add_f32 v1, v1, v255\n"
    "v_mfma_f32_16x16x32_bf16 v[44:47], a[160:163], a[120:123], v[44:47]\n"
    "ds_read_b64_tr_b16 a[110:111], v233 offset:21952\n"
    "buffer_load_dwordx4 v229, s[24:27], s64 offen offset:0 lds\n"
    "v_rcp_f32 v252, v252\n"
    "v_mfma_f32_16x16x32_bf16 v[48:51], a[164:167], a[120:123], v[48:51]\n"
    "s_waitcnt lgkmcnt(4)\n"
    "v_rcp_f32 v1, v1\n"
    "v_mul_f32 v160, v160, v252\n"
    "v_mfma_f32_16x16x32_bf16 v[52:55], a[156:159], a[124:127], v[52:55]\n"
    "ds_read_b64_tr_b16 a[112:113], v234 offset:20704\n"
    "v_mul_f32 v172, v172, v1\n"
    "v_mul_f32 v161, v161, v252\n"
    "v_mfma_f32_16x16x32_bf16 v[56:59], a[160:163], a[124:127], v[56:59]\n"
    "ds_read_b64_tr_b16 a[114:115], v234 offset:21984\n"
    "buffer_load_dwordx4 v230, s[24:27], s64 offen offset:1024 lds\n"
    "v_mul_f32 v173, v173, v1\n"
    "v_mfma_f32_16x16x32_bf16 v[60:63], a[164:167], a[124:127], v[60:63]\n"
    "s_waitcnt lgkmcnt(4)\n"
    "v_mul_f32 v162, v162, v252\n"
    "v_mul_f32 v174, v174, v1\n"
    "v_mul_f32 v163, v163, v252\n"
    "v_mul_f32 v175, v175, v1\n"
    "v_mfma_f32_16x16x32_bf16 v[64:67], a[156:159], a[128:131], v[64:67]\n"
    "ds_read_b64_tr_b16 a[116:117], v233 offset:20736\n"
    "v_mul_f32 v164, v164, v252\n"
    "v_mul_f32 v176, v176, v1\n"
    "v_mfma_f32_16x16x32_bf16 v[68:71], a[160:163], a[128:131], v[68:71]\n"
    "s_add_u32 s63, s66, s99\n"
    "ds_read_b64_tr_b16 a[118:119], v233 offset:22016\n"
    "v_mul_f32 v165, v165, v252\n"
    "v_mfma_f32_16x16x32_bf16 v[72:75], a[164:167], a[128:131], v[72:75]\n"
    "s_waitcnt lgkmcnt(4)\n"
    "v_mul_f32 v177, v177, v1\n"
    "v_mul_f32 v166, v166, v252\n"
    "v_mfma_f32_16x16x32_bf16 v[76:79], a[156:159], a[108:111], v[76:79]\n"
    "s_add_u32 m0, s60, 12288\n"
    "ds_read_b64_tr_b16 a[120:121], v233 offset:30720\n"
    "buffer_load_dwordx4 v220, s[16:19], s63 offen offset:0 lds\n"
    "v_mul_f32 v178, v178, v1\n"
    "v_mul_f32 v167, v167, v252\n"
    "v_mul_f32 v179, v179, v1\n"
    "v_mfma_f32_16x16x32_bf16 v[80:83], a[160:163], a[108:111], v[80:83]\n"
    "ds_read_b64_tr_b16 a[122:123], v233 offset:32000\n"
    "v_mul_f32 v168, v168, v252\n"
    "v_mul_f32 v180, v180, v1\n"
    "v_mfma_f32_16x16x32_bf16 v[84:87], a[164:167], a[108:111], v[84:87]\n"
    "s_waitcnt lgkmcnt(4)\n"
    "buffer_load_dwordx4 v221, s[16:19], s63 offen offset:1024 lds\n"
    "v_mul_f32 v169, v169, v252\n"
    "v_mul_f32 v181, v181, v1\n"
    "v_mfma_f32_16x16x32_bf16 v[88:91], a[156:159], a[112:115], v[88:91]\n"
    "ds_read_b64_tr_b16 a[124:125], v234 offset:30752\n"
    "v_mul_f32 v170, v170, v252\n"
    "v_mul_f32 v182, v182, v1\n"
    "v_mfma_f32_16x16x32_bf16 v[92:95], a[160:163], a[112:115], v[92:95]\n"
    "ds_read_b64_tr_b16 a[126:127], v234 offset:32032\n"
    "v_mul_f32 v171, v171, v252\n"
    "v_mul_f32 v183, v183, v1\n"
    "v_cvt_pk_bf16_f32 v184, v160, v161\n"
    "v_mfma_f32_16x16x32_bf16 v[96:99], a[164:167], a[112:115], v[96:99]\n"
    "s_waitcnt lgkmcnt(4)\n"
    "buffer_load_dwordx4 v222, s[16:19], s63 offen offset:2048 lds\n"
    "v_cvt_pk_bf16_f32 v196, v172, v173\n"
    "v_cvt_pk_bf16_f32 v185, v162, v163\n"
    "v_mfma_f32_16x16x32_bf16 v[100:103], a[156:159], a[116:119], v[100:103]\n"
    "s_add_u32 m0, s60, 15360\n"
    "ds_read_b64_tr_b16 a[128:129], v233 offset:30784\n"
    "v_cvt_pk_bf16_f32 v197, v174, v175\n"
    "v_cvt_pk_bf16_f32 v186, v164, v165\n"
    "v_mfma_f32_16x16x32_bf16 v[104:107], a[160:163], a[116:119], v[104:107]\n"
    "ds_read_b64_tr_b16 a[130:131], v233 offset:32064\n"
    "v_cvt_pk_bf16_f32 v198, v176, v177\n"
    "v_cvt_pk_bf16_f32 v187, v166, v167\n"
    "v_mfma_f32_16x16x32_bf16 v[108:111], a[164:167], a[116:119], v[108:111]\n"
    "s_waitcnt lgkmcnt(4)\n"
    "buffer_load_dwordx4 v223, s[16:19], s63 offen offset:0 lds\n"
    "v_cvt_pk_bf16_f32 v199, v178, v179\n"
    "v_cvt_pk_bf16_f32 v188, v168, v169\n"
    "v_cvt_pk_bf16_f32 v200, v180, v181\n"
    "v_mfma_f32_16x16x32_bf16 v[4:7], a[168:171], a[120:123], v[4:7]\n"
    "ds_read_b64_tr_b16 a[108:109], v234 offset:30816\n"
    "v_cvt_pk_bf16_f32 v189, v170, v171\n"
    "v_mfma_f32_16x16x32_bf16 v[8:11], a[172:175], a[120:123], v[8:11]\n"
    "ds_read_b64_tr_b16 a[110:111], v234 offset:32096\n"
    "v_cvt_pk_bf16_f32 v201, v182, v183\n"
    "v_mfma_f32_16x16x32_bf16 v[12:15], a[176:179], a[120:123], v[12:15]\n"
    "s_waitcnt lgkmcnt(4)\n"
    "buffer_store_dwordx4 v[184:187], v237, s[28:31], s73 offen offset:0 sc1\n"
    "buffer_store_dwordx2 v[188:189], v238, s[28:31], s73 offen offset:0 sc1\n"
    "v_mfma_f32_16x16x32_bf16 v[16:19], a[168:171], a[124:127], v[16:19]\n"
    "s_waitcnt vmcnt(11)\n"
    "ds_read_b64_tr_b16 a[112:113], v233 offset:30848\n"
    "ds_read_b128 a[180:183], v231 offset:6144\n"
    "buffer_store_dwordx4 v[196:199], v237, s[28:31], s73 offen offset:1024 sc1\n"
    "buffer_store_dwordx2 v[200:201], v238, s[28:31], s73 offen offset:512 sc1\n"
    "v_mfma_f32_16x16x32_bf16 v[20:23], a[172:175], a[124:127], v[20:23]\n"
    "ds_read_b64_tr_b16 a[114:115], v233 offset:32128\n"
    "ds_read_b128 a[184:187], v231 offset:8192\n"
    "v_max3_f32 v250, v192, v193, v194\n"
    "v_max3_f32 v251, v195, v204, v205\n"
    "v_mfma_f32_16x16x32_bf16 v[24:27], a[176:179], a[124:127], v[24:27]\n"
    "s_waitcnt lgkmcnt(6)\n"
    "ds_read_b128 a[188:191], v231 offset:10240\n"
    "buffer_load_dwordx4 v224, s[16:19], s63 offen offset:1024 lds\n"
    "v_max3_f32 v252, v206, v207, v216\n"
    "v_max3_f32 v253, v217, v218, v219\n"
    "v_mfma_f32_16x16x32_bf16 v[28:31], a[168:171], a[128:131], v[28:31]\n"
    "ds_read_b64_tr_b16 a[116:117], v234 offset:30880\n"
    "ds_read_b128 a[192:195], v232 offset:6144\n"
    "v_max3_f32 v250, v250, v251, v252\n"
    "v_max_f32 v250, v250, v253\n"
    "v_mfma_f32_16x16x32_bf16 v[32:35], a[172:175], a[128:131], v[32:35]\n"
    "ds_read_b64_tr_b16 a[118:119], v234 offset:32160\n"
    "buffer_load_dwordx4 v225, s[16:19], s63 offen offset:2048 lds\n"
    "v_mov_b32 v251, v250\n"
    "v_mfma_f32_16x16x32_bf16 v[36:39], a[176:179], a[128:131], v[36:39]\n"
    "s_waitcnt lgkmcnt(8)\n"
    "ds_read_b128 a[196:199], v232 offset:8192\n"
    "v_cvt_pk_bf16_f32 v112, v4, v5\n"
    "v_permlane32_swap_b32 v250, v251\n"
    "v_max_f32 v250, v250, v251\n"
    "v_mov_b32 v251, v250\n"
    "v_mfma_f32_16x16x32_bf16 v[40:43], a[168:171], a[108:111], v[40:43]\n"
    "ds_read_b64_tr_b16 a[120:121], v233 offset:30912\n"
    "v_cvt_pk_bf16_f32 v113, v6, v7\n"
    "v_permlane16_swap_b32 v250, v251\n"
    "v_mfma_f32_16x16x32_bf16 v[44:47], a[172:175], a[108:111], v[44:47]\n"
    "ds_read_b64_tr_b16 a[122:123], v233 offset:32192\n"
    "ds_read_b128 a[200:203], v232 offset:10240\n"
    "v_max_f32 v250, v250, v251\n"
    "v_mul_f32 v250, s80, v250\n"
    "v_mfma_f32_16x16x32_bf16 v[48:51], a[176:179], a[108:111], v[48:51]\n"
    "s_waitcnt lgkmcnt(9)\n"
    "v_cvt_pk_bf16_f32 v114, v8, v9\n"
    "v_fma_f32 v192, v192, s80, -v250\n"
    "v_fma_f32 v193, v193, s80, -v250\n"
    "v_mfma_f32_16x16x32_bf16 v[52:55], a[168:171], a[112:115], v[52:55]\n"
    "ds_read_b64_tr_b16 a[124:125], v234 offset:30944\n"
    "v_cvt_pk_bf16_f32 v115, v10, v11\n"
    "v_exp_f32 v192, v192\n"
    "v_fma_f32 v194, v194, s80, -v250\n"
    "v_mfma_f32_16x16x32_bf16 v[56:59], a[172:175], a[112:115], v[56:59]\n"
    "ds_read_b64_tr_b16 a[126:127], v234 offset:32224\n"
    "v_cvt_pk_bf16_f32 v116, v12, v13\n"
    "v_exp_f32 v193, v193\n"
    "v_mfma_f32_16x16x32_bf16 v[60:63], a[176:179], a[112:115], v[60:63]\n"
    "s_waitcnt lgkmcnt(6)\n"
    "v_cvt_pk_bf16_f32 v117, v14, v15\n"
    "v_fma_f32 v195, v195, s80, -v250\n"
    "v_mfma_f32_16x16x32_bf16 v[64:67], a[168:171], a[116:119], v[64:67]\n"
    "ds_read_b64_tr_b16 a[128:129], v233 offset:30976\n"
    "v_cvt_pk_bf16_f32 v120, v16, v17\n"
    "v_exp_f32 v194, v194\n"
    "v_fma_f32 v204, v204, s80, -v250\n"
    "v_mfma_f32_16x16x32_bf16 v[68:71], a[172:175], a[116:119], v[68:71]\n"
    "ds_read_b64_tr_b16 a[130:131], v233 offset:32256\n"
    "s_barrier\n"
    "v_exp_f32 v195, v195\n"
    "v_fma_f32 v205, v205, s80, -v250\n"
    "v_mfma_f32_16x16x32_bf16 v[72:75], a[176:179], a[116:119], v[72:75]\n"
    "s_waitcnt lgkmcnt(5)\n"
    "v_cvt_pk_bf16_f32 v121, v18, v19\n"
    "v_exp_f32 v204, v204\n"
    "v_mfma_f32_16x16x32_bf16 v[76:79], a[168:171], a[120:123], v[76:79]\n"
    "ds_read_b64_tr_b16 a[108:109], v233 offset:40960\n"
    "v_cvt_pk_bf16_f32 v122, v20, v21\n"
    "v_fma_f32 v206, v206, s80, -v250\n"
    "v_mfma_f32_16x16x32_bf16 v[80:83], a[172:175], a[120:123], v[80:83]\n"
    "ds_read_b64_tr_b16 a[110:111], v233 offset:42240\n"
    "v_cvt_pk_bf16_f32 v123, v22, v23\n"
    "v_exp_f32 v205, v205\n"
    "v_fma_f32 v207, v207, s80, -v250\n"
    "v_mfma_f32_16x16x32_bf16 v[84:87], a[176:179], a[120:123], v[84:87]\n"
    "s_waitcnt lgkmcnt(4)\n"
    "v_cvt_pk_bf16_f32 v124, v24, v25\n"
    "v_exp_f32 v206, v206\n"
    "v_fma_f32 v216, v216, s80, -v250\n"
    "v_mfma_f32_16x16x32_bf16 v[88:91], a[168:171], a[124:127], v[88:91]\n"
    "ds_read_b64_tr_b16 a[112:113], v234 offset:40992\n"
    "v_cvt_pk_bf16_f32 v125, v26, v27\n"
    "v_exp_f32 v207, v207\n"
    "v_mfma_f32_16x16x32_bf16 v[92:95], a[172:175], a[124:127], v[92:95]\n"
    "ds_read_b64_tr_b16 a[114:115], v234 offset:42272\n"
    "v_cvt_pk_bf16_f32 v128, v28, v29\n"
    "v_fma_f32 v217, v217, s80, -v250\n"
    "v_mfma_f32_16x16x32_bf16 v[96:99], a[176:179], a[124:127], v[96:99]\n"
    "s_waitcnt lgkmcnt(4)\n"
    "v_cvt_pk_bf16_f32 v129, v30, v31\n"
    "v_exp_f32 v216, v216\n"
    "v_fma_f32 v218, v218, s80, -v250\n"
    "v_mfma_f32_16x16x32_bf16 v[100:103], a[168:171], a[128:131], v[100:103]\n"
    "ds_read_b64_tr_b16 a[116:117], v233 offset:41024\n"
    "v_cvt_pk_bf16_f32 v130, v32, v33\n"
    "v_exp_f32 v217, v217\n"
    "v_fma_f32 v219, v219, s80, -v250\n"
    "v_mfma_f32_16x16x32_bf16 v[104:107], a[172:175], a[128:131], v[104:107]\n"
    "ds_read_b64_tr_b16 a[118:119], v233 offset:42304\n"
    "v_cvt_pk_bf16_f32 v131, v34, v35\n"
    "v_exp_f32 v218, v218\n"
    "v_mfma_f32_16x16x32_bf16 v[108:111], a[176:179], a[128:131], v[108:111]\n"
    "s_waitcnt lgkmcnt(4)\n"
    "v_cvt_pk_bf16_f32 v132, v36, v37\n"
    "v_exp_f32 v219, v219\n"
    "v_mfma_f32_16x16x32_bf16 v[4:7], a[180:183], a[108:111], 0\n"
    "ds_read_b64_tr_b16 a[120:121], v234 offset:41056\n"
    "v_cvt_pk_bf16_f32 v133, v38, v39\n"
    "v_add_f32 v251, v192, v193\n"
    "v_add_f32 v252, v194, v195\n"
    "v_mfma_f32_16x16x32_bf16 v[8:11], a[184:187], a[108:111], 0\n"
    "ds_read_b64_tr_b16 a[122:123], v234 offset:42336\n"
    "v_cvt_pk_bf16_f32 v136, v40, v41\n"
    "v_add_f32 v253, v204, v205\n"
    "v_add_f32 v251, v251, v206\n"
    "v_add_f32 v252, v252, v207\n"
    "v_mfma_f32_16x16x32_bf16 v[12:15], a[188:191], a[108:111], 0\n"
    "s_waitcnt lgkmcnt(4)\n"
    "v_cvt_pk_bf16_f32 v137, v42, v43\n"
    "v_add_f32 v253, v253, v216\n"
    "v_add_f32 v251, v251, v217\n"
    "v_mfma_f32_16x16x32_bf16 v[16:19], a[180:183], a[112:115], 0\n"
    "s_add_u32 s64, s71, s46\n"
    "ds_read_b64_tr_b16 a[124:125], v233 offset:41088\n"
    "v_cvt_pk_bf16_f32 v138, v44, v45\n"
    "v_add_f32 v252, v252, v218\n"
    "v_add_f32 v253, v253, v219\n"
    "v_mfma_f32_16x16x32_bf16 v[20:23], a[184:187], a[112:115], 0\n"
    "s_add_u32 m0, s50, 65536\n"
    "ds_read_b64_tr_b16 a[126:127], v233 offset:42368\n"
    "buffer_load_dwordx4 v226, s[24:27], s64 offen offset:0 lds\n"
    "v_add_f32 v252, v251, v252\n"
    "v_add_f32 v252, v252, v253\n"
    "v_mfma_f32_16x16x32_bf16 v[24:27], a[188:191], a[112:115], 0\n"
    "s_waitcnt lgkmcnt(4)\n"
    "buffer_load_dwordx4 v227, s[24:27], s64 offen offset:1024 lds\n"
    "v_cvt_pk_bf16_f32 v139, v46, v47\n"
    "v_mov_b32 v251, v252\n"
    "s_nop 1\n"
    "v_permlane32_swap_b32 v252, v251\n"
    "v_mfma_f32_16x16x32_bf16 v[28:31], a[180:183], a[116:119], 0\n"
    "ds_read_b64_tr_b16 a[128:129], v234 offset:41120\n"
    "buffer_load_dwordx4 v228, s[24:27], s64 offen offset:2048 lds\n"
    "v_add_f32 v252, v252, v251\n"
    "v_mov_b32 v251, v252\n"
    "v_mfma_f32_16x16x32_bf16 v[32:35], a[184:187], a[116:119], 0\n"
    "ds_read_b64_tr_b16 a[130:131], v234 offset:42400\n"
    "v_cvt_pk_bf16_f32 v140, v48, v49\n"
    "v_cvt_pk_bf16_f32 v141, v50, v51\n"
    "v_permlane16_swap_b32 v252, v251\n"
    "v_mfma_f32_16x16x32_bf16 v[36:39], a[188:191], a[116:119], 0\n"
    "s_waitcnt lgkmcnt(4)\n"
    "v_cvt_pk_bf16_f32 v144, v52, v53\n"
    "v_add_f32 v252, v252, v251\n"
    "v_mfma_f32_16x16x32_bf16 v[40:43], a[180:183], a[120:123], 0\n"
    "s_add_u32 m0, s50, 68608\n"
    "ds_read_b64_tr_b16 a[108:109], v233 offset:41152\n"
    "v_cvt_pk_bf16_f32 v145, v54, v55\n"
    "v_rcp_f32 v252, v252\n"
    "s_nop 0\n"
    "v_mul_f32 v192, v192, v252\n"
    "v_mul_f32 v193, v193, v252\n"
    "v_mfma_f32_16x16x32_bf16 v[44:47], a[184:187], a[120:123], 0\n"
    "ds_read_b64_tr_b16 a[110:111], v233 offset:42432\n"
    "buffer_load_dwordx4 v229, s[24:27], s64 offen offset:0 lds\n"
    "v_cvt_pk_bf16_f32 v146, v56, v57\n"
    "v_cvt_pk_bf16_f32 v147, v58, v59\n"
    "v_cvt_pk_bf16_f32 v148, v60, v61\n"
    "v_cvt_pk_bf16_f32 v149, v62, v63\n"
    "v_mul_f32 v194, v194, v252\n"
    "v_mul_f32 v195, v195, v252\n"
    "v_mfma_f32_16x16x32_bf16 v[48:51], a[188:191], a[120:123], 0\n"
    "s_waitcnt lgkmcnt(4)\n"
    "v_cvt_pk_bf16_f32 v152, v64, v65\n"
    "v_mul_f32 v204, v204, v252\n"
    "v_mul_f32 v205, v205, v252\n"
    "v_mfma_f32_16x16x32_bf16 v[52:55], a[180:183], a[124:127], 0\n"
    "ds_read_b64_tr_b16 a[112:113], v234 offset:41184\n"
    "v_cvt_pk_bf16_f32 v153, v66, v67\n"
    "v_mul_f32 v206, v206, v252\n"
    "v_mul_f32 v207, v207, v252\n"
    "v_mfma_f32_16x16x32_bf16 v[56:59], a[184:187], a[124:127], 0\n"
    "ds_read_b64_tr_b16 a[114:115], v234 offset:42464\n"
    "buffer_load_dwordx4 v230, s[24:27], s64 offen offset:1024 lds\n"
    "v_cvt_pk_bf16_f32 v154, v68, v69\n"
    "v_cvt_pk_bf16_f32 v155, v70, v71\n"
    "v_cvt_pk_bf16_f32 v156, v72, v73\n"
    "v_cvt_pk_bf16_f32 v157, v74, v75\n"
    "v_mul_f32 v216, v216, v252\n"
    "v_mul_f32 v217, v217, v252\n"
    "v_mul_f32 v218, v218, v252\n"
    "v_mfma_f32_16x16x32_bf16 v[60:63], a[188:191], a[124:127], 0\n"
    "s_waitcnt lgkmcnt(4)\n"
    "v_cvt_pk_bf16_f32 v160, v76, v77\n"
    "v_mul_f32 v219, v219, v252\n"
    "v_cvt_pk_bf16_f32 v208, v192, v193\n"
    "v_mfma_f32_16x16x32_bf16 v[64:67], a[180:183], a[128:131], 0\n"
    "ds_read_b64_tr_b16 a[116:117], v233 offset:41216\n"
    "v_cvt_pk_bf16_f32 v161, v78, v79\n"
    "v_cvt_pk_bf16_f32 v209, v194, v195\n"
    "v_cvt_pk_bf16_f32 v210, v204, v205\n"
    "v_mfma_f32_16x16x32_bf16 v[68:71], a[184:187], a[128:131], 0\n"
    "s_add_u32 s63, s67, s98\n"
    "ds_read_b64_tr_b16 a[118:119], v233 offset:42496\n"
    "v_cvt_pk_bf16_f32 v162, v80, v81\n"
    "v_cvt_pk_bf16_f32 v163, v82, v83\n"
    "v_cvt_pk_bf16_f32 v164, v84, v85\n"
    "v_cvt_pk_bf16_f32 v165, v86, v87\n"
    "v_cvt_pk_bf16_f32 v211, v206, v207\n"
    "v_cvt_pk_bf16_f32 v212, v216, v217\n"
    "v_mfma_f32_16x16x32_bf16 v[72:75], a[188:191], a[128:131], 0\n"
    "s_waitcnt lgkmcnt(4)\n"
    "v_cvt_pk_bf16_f32 v168, v88, v89\n"
    "v_cvt_pk_bf16_f32 v213, v218, v219\n"
    "buffer_store_dwordx4 v[208:211], v237, s[28:31], s73 offen offset:2048 sc1\n"
    "buffer_store_dwordx2 v[212:213], v238, s[28:31], s73 offen offset:1024 sc1\n"
    "v_mfma_f32_16x16x32_bf16 v[76:79], a[180:183], a[108:111], 0\n"
    "s_add_u32 m0, s60, 0\n"
    "ds_read_b64_tr_b16 a[120:121], v233 offset:51200\n"
    "buffer_load_dwordx4 v220, s[16:19], s63 offen offset:0 lds\n"
    "v_mfma_f32_16x16x32_bf16 v[80:83], a[184:187], a[108:111], 0\n"
    "ds_read_b64_tr_b16 a[122:123], v233 offset:52480\n"
    "v_cvt_pk_bf16_f32 v169, v90, v91\n"
    "v_cvt_pk_bf16_f32 v170, v92, v93\n"
    "v_cvt_pk_bf16_f32 v171, v94, v95\n"
    "v_cvt_pk_bf16_f32 v172, v96, v97\n"
    "v_cvt_pk_bf16_f32 v173, v98, v99\n"
    "v_mfma_f32_16x16x32_bf16 v[84:87], a[188:191], a[108:111], 0\n"
    "s_waitcnt lgkmcnt(4)\n"
    "buffer_load_dwordx4 v221, s[16:19], s63 offen offset:1024 lds\n"
    "v_cvt_pk_bf16_f32 v176, v100, v101\n"
    "v_mfma_f32_16x16x32_bf16 v[88:91], a[180:183], a[112:115], 0\n"
    "ds_read_b64_tr_b16 a[124:125], v234 offset:51232\n"
    "v_cvt_pk_bf16_f32 v177, v102, v103\n"
    "v_mfma_f32_16x16x32_bf16 v[92:95], a[184:187], a[112:115], 0\n"
    "ds_read_b64_tr_b16 a[126:127], v234 offset:52512\n"
    "v_cvt_pk_bf16_f32 v178, v104, v105\n"
    "v_cvt_pk_bf16_f32 v179, v106, v107\n"
    "v_cvt_pk_bf16_f32 v180, v108, v109\n"
    "v_cvt_pk_bf16_f32 v181, v110, v111\n"
    "v_mfma_f32_16x16x32_bf16 v[96:99], a[188:191], a[112:115], 0\n"
    "s_waitcnt lgkmcnt(4)\n"
    "buffer_load_dwordx4 v222, s[16:19], s63 offen offset:2048 lds\n"
    "v_mfma_f32_16x16x32_bf16 v[100:103], a[180:183], a[116:119], 0\n"
    "s_add_u32 m0, s60, 3072\n"
    "ds_read_b64_tr_b16 a[128:129], v233 offset:51264\n"
    "v_mfma_f32_16x16x32_bf16 v[104:107], a[184:187], a[116:119], 0\n"
    "ds_read_b64_tr_b16 a[130:131], v233 offset:52544\n"
    "v_mfma_f32_16x16x32_bf16 v[108:111], a[188:191], a[116:119], 0\n"
    "s_waitcnt lgkmcnt(4)\n"
    "buffer_load_dwordx4 v223, s[16:19], s63 offen offset:0 lds\n"
    "v_mfma_f32_16x16x32_bf16 v[4:7], a[192:195], a[120:123], v[4:7]\n"
    "s_waitcnt vmcnt(11)\n"
    "ds_read_b64_tr_b16 a[108:109], v234 offset:51296\n"
    "v_mfma_f32_16x16x32_bf16 v[8:11], a[196:199], a[120:123], v[8:11]\n"
    "ds_read_b64_tr_b16 a[110:111], v234 offset:52576\n"
    "ds_read_b128 a[132:135], v231 offset:12288\n"
    "v_mfma_f32_16x16x32_bf16 v[12:15], a[200:203], a[120:123], v[12:15]\n"
    "s_waitcnt lgkmcnt(5)\n"
    "ds_read_b128 a[136:139], v231 offset:14336\n"
    "v_mfma_f32_16x16x32_bf16 v[16:19], a[192:195], a[124:127], v[16:19]\n"
    "ds_read_b64_tr_b16 a[112:113], v233 offset:51328\n"
    "buffer_load_dwordx4 v224, s[16:19], s63 offen offset:1024 lds\n"
    "v_mfma_f32_16x16x32_bf16 v[20:23], a[196:199], a[124:127], v[20:23]\n"
    "ds_read_b64_tr_b16 a[114:115], v233 offset:52608\n"
    "ds_read_b128 a[140:143], v231 offset:16384\n"
    "v_mfma_f32_16x16x32_bf16 v[24:27], a[200:203], a[124:127], v[24:27]\n"
    "s_waitcnt lgkmcnt(7)\n"
    "v_mfma_f32_16x16x32_bf16 v[28:31], a[192:195], a[128:131], v[28:31]\n"
    "ds_read_b64_tr_b16 a[116:117], v234 offset:51360\n"
    "ds_read_b128 a[144:147], v232 offset:12288\n"
    "v_mfma_f32_16x16x32_bf16 v[32:35], a[196:199], a[128:131], v[32:35]\n"
    "ds_read_b64_tr_b16 a[118:119], v234 offset:52640\n"
    "buffer_load_dwordx4 v225, s[16:19], s63 offen offset:2048 lds\n"
    "v_mfma_f32_16x16x32_bf16 v[36:39], a[200:203], a[128:131], v[36:39]\n"
    "s_waitcnt lgkmcnt(8)\n"
    "ds_read_b128 a[148:151], v232 offset:14336\n"
    "v_mfma_f32_16x16x32_bf16 v[40:43], a[192:195], a[108:111], v[40:43]\n"
    "ds_read_b64_tr_b16 a[120:121], v233 offset:51392\n"
    "v_mfma_f32_16x16x32_bf16 v[44:47], a[196:199], a[108:111], v[44:47]\n"
    "ds_read_b64_tr_b16 a[122:123], v233 offset:52672\n"
    "ds_read_b128 a[152:155], v232 offset:16384\n"
    "v_mfma_f32_16x16x32_bf16 v[48:51], a[200:203], a[108:111], v[48:51]\n"
    "s_waitcnt lgkmcnt(8)\n"
    "v_mfma_f32_16x16x32_bf16 v[52:55], a[192:195], a[112:115], v[52:55]\n"
    "ds_read_b64_tr_b16 a[124:125], v234 offset:51424\n"
    "v_mfma_f32_16x16x32_bf16 v[56:59], a[196:199], a[112:115], v[56:59]\n"
    "ds_read_b64_tr_b16 a[126:127], v234 offset:52704\n"
    "v_mfma_f32_16x16x32_bf16 v[60:63], a[200:203], a[112:115], v[60:63]\n"
    "s_waitcnt lgkmcnt(6)\n"
    "v_mfma_f32_16x16x32_bf16 v[64:67], a[192:195], a[116:119], v[64:67]\n"
    "ds_read_b64_tr_b16 a[128:129], v233 offset:51456\n"
    "v_mfma_f32_16x16x32_bf16 v[68:71], a[196:199], a[116:119], v[68:71]\n"
    "ds_read_b64_tr_b16 a[130:131], v233 offset:52736\n"
    "s_barrier\n"
    "v_mfma_f32_16x16x32_bf16 v[72:75], a[200:203], a[116:119], v[72:75]\n"
    "s_waitcnt lgkmcnt(5)\n"
    "s_add_u32 s64, s71, s47\n"
    "s_add_u32 m0, s50, 20480\n"
    "v_mfma_f32_16x16x32_bf16 v[76:79], a[192:195], a[120:123], v[76:79]\n"
    "ds_read_b64_tr_b16 a[108:109], v233 offset:0\n"
    "buffer_load_dwordx4 v226, s[24:27], s64 offen offset:0 lds\n"
    "v_mfma_f32_16x16x32_bf16 v[80:83], a[196:199], a[120:123], v[80:83]\n"
    "ds_read_b64_tr_b16 a[110:111], v233 offset:1280\n"
    "v_mfma_f32_16x16x32_bf16 v[84:87], a[200:203], a[120:123], v[84:87]\n"
    "s_waitcnt lgkmcnt(4)\n"
    "buffer_load_dwordx4 v227, s[24:27], s64 offen offset:1024 lds\n"
    "v_mfma_f32_16x16x32_bf16 v[88:91], a[192:195], a[124:127], v[88:91]\n"
    "ds_read_b64_tr_b16 a[112:113], v234 offset:32\n"
    "v_mfma_f32_16x16x32_bf16 v[92:95], a[196:199], a[124:127], v[92:95]\n"
    "ds_read_b64_tr_b16 a[114:115], v234 offset:1312\n"
    "v_mfma_f32_16x16x32_bf16 v[96:99], a[200:203], a[124:127], v[96:99]\n"
    "s_waitcnt lgkmcnt(4)\n"
    "buffer_load_dwordx4 v228, s[24:27], s64 offen offset:2048 lds\n"
    "v_mfma_f32_16x16x32_bf16 v[100:103], a[192:195], a[128:131], v[100:103]\n"
    "ds_read_b64_tr_b16 a[116:117], v233 offset:64\n"
    "v_mfma_f32_16x16x32_bf16 v[104:107], a[196:199], a[128:131], v[104:107]\n"
    "s_add_u32 m0, s50, 23552\n"
    "ds_read_b64_tr_b16 a[118:119], v233 offset:1344\n"
    "v_mfma_f32_16x16x32_bf16 v[108:111], a[200:203], a[128:131], v[108:111]\n"
    "v_mfma_f32_16x16x32_bf16 a[0:3], v[112:115], v[184:187], 0\n"
    "s_cmp_lt_u32 s57, 4\n"
    "s_cselect_b32 s52, 2, 1\n"
    "buffer_load_dwordx4 v229, s[24:27], s64 offen offset:0 lds\n"
    "v_mfma_f32_16x16x32_bf16 a[4:7], v[112:115], v[196:199], 0\n"
    "s_lshl_b32 s52, s52, 17\n"
    "s_sub_u32 s8, s57, 1\n"
    "s_and_b32 s8, s8, 7\n"
    "v_mfma_f32_16x16x32_bf16 a[8:11], v[112:115], v[208:211], 0\n"
    "s_add_u32 s8, s8, 0\n"
    "s_lshl_b32 s8, s8, 10\n"
    "s_add_u32 s8, s8, s77\n"
    "v_mfma_f32_16x16x32_bf16 a[12:15], v[120:123], v[184:187], 0\n"
    "s_cmp_eq_u32 s57, 0\n"
    "s_cselect_b32 s8, 2147418112, s8\n"
    "buffer_load_dwordx4 v230, s[24:27], s64 offen offset:1024 lds\n"
    "v_mfma_f32_16x16x32_bf16 a[16:19], v[120:123], v[196:199], 0\n"
    "s_sub_u32 s9, s57, 2\n"
    "s_and_b32 s9, s9, 7\n"
    "s_add_u32 s9, s9, 8\n"
    "v_mfma_f32_16x16x32_bf16 a[20:23], v[120:123], v[208:211], 0\n"
    "s_lshl_b32 s9, s9, 10\n"
    "s_add_u32 s9, s9, s77\n"
    "v_mfma_f32_16x16x32_bf16 a[24:27], v[128:131], v[184:187], 0\n"
    "s_add_u32 s63, s67, s99\n"
    "s_add_u32 m0, s60, 6144\n"
    "s_nop 0\n"
    "buffer_load_dwordx4 v220, s[16:19], s63 offen offset:0 lds\n"
    "v_mfma_f32_16x16x32_bf16 a[28:31], v[128:131], v[196:199], 0\n"
    "s_cmp_eq_u32 s57, 1\n"
    "s_cselect_b32 s9, 2147418112, s9\n"
    "s_sub_u32 s10, s57, 3\n"
    "v_mfma_f32_16x16x32_bf16 a[32:35], v[128:131], v[208:211], 0\n"
    "s_and_b32 s10, s10, 7\n"
    "s_add_u32 s10, s10, 16\n"
    "s_lshl_b32 s10, s10, 10\n"
    "v_mfma_f32_16x16x32_bf16 a[36:39], v[136:139], v[184:187], 0\n"
    "s_add_u32 s10, s10, s77\n"
    "s_cmp_eq_u32 s57, 2\n"
    "s_cselect_b32 s10, 2147418112, s10\n"
    "v_mfma_f32_16x16x32_bf16 a[40:43], v[136:139], v[196:199], 0\n"
    "s_sub_u32 s11, s57, 4\n"
    "s_and_b32 s11, s11, 7\n"
    "buffer_load_dwordx4 v221, s[16:19], s63 offen offset:1024 lds\n"
    "v_mfma_f32_16x16x32_bf16 a[44:47], v[136:139], v[208:211], 0\n"
    "s_add_u32 s11, s11, 24\n"
    "s_lshl_b32 s11, s11, 10\n"
    "s_add_u32 s11, s11, s77\n"
    "v_mfma_f32_16x16x32_bf16 a[48:51], v[144:147], v[184:187], 0\n"
    "s_cmp_eq_u32 s57, 3\n"
    "s_cselect_b32 s11, 2147418112, s11\n"
    "s_sub_u32 s12, s57, 5\n"
    "v_mfma_f32_16x16x32_bf16 a[52:55], v[144:147], v[196:199], 0\n"
    "s_and_b32 s12, s12, 7\n"
    "s_add_u32 s12, s12, 32\n"
    "buffer_load_dwordx4 v222, s[16:19], s63 offen offset:2048 lds\n"
    "v_mfma_f32_16x16x32_bf16 a[56:59], v[144:147], v[208:211], 0\n"
    "s_add_u32 m0, s60, 9216\n"
    "s_lshl_b32 s12, s12, 10\n"
    "s_add_u32 s12, s12, s77\n"
    "v_mfma_f32_16x16x32_bf16 a[60:63], v[152:155], v[184:187], 0\n"
    "s_cmp_eq_u32 s57, 4\n"
    "s_cselect_b32 s12, 2147418112, s12\n"
    "s_sub_u32 s13, s57, 6\n"
    "v_mfma_f32_16x16x32_bf16 a[64:67], v[152:155], v[196:199], 0\n"
    "s_and_b32 s13, s13, 7\n"
    "s_add_u32 s13, s13, 40\n"
    "buffer_load_dwordx4 v223, s[16:19], s63 offen offset:0 lds\n"
    "v_mfma_f32_16x16x32_bf16 a[68:71], v[152:155], v[208:211], 0\n"
    "s_lshl_b32 s13, s13, 10\n"
    "s_add_u32 s13, s13, s77\n"
    "v_mfma_f32_16x16x32_bf16 a[72:75], v[160:163], v[184:187], 0\n"
    "s_cmp_eq_u32 s57, 5\n"
    "s_cselect_b32 s13, 2147418112, s13\n"
    "s_sub_u32 s14, s57, 5\n"
    "v_mfma_f32_16x16x32_bf16 a[76:79], v[160:163], v[196:199], 0\n"
    "s_and_b32 s14, s14, 7\n"
    "s_add_u32 s14, s14, 48\n"
    "s_lshl_b32 s14, s14, 10\n"
    "v_mfma_f32_16x16x32_bf16 a[80:83], v[160:163], v[208:211], 0\n"
    "s_add_u32 s14, s14, s77\n"
    "buffer_load_dwordx4 v224, s[16:19], s63 offen offset:1024 lds\n"
    "v_mfma_f32_16x16x32_bf16 a[84:87], v[168:171], v[184:187], 0\n"
    "s_cmp_eq_u32 s57, 4\n"
    "s_cselect_b32 s14, 2147418112, s14\n"
    "s_sub_u32 s15, s57, 6\n"
    "v_mfma_f32_16x16x32_bf16 a[88:91], v[168:171], v[196:199], 0\n"
    "s_and_b32 s15, s15, 7\n"
    "s_add_u32 s15, s15, 56\n"
    "s_lshl_b32 s15, s15, 10\n"
    "v_mfma_f32_16x16x32_bf16 a[92:95], v[168:171], v[208:211], 0\n"
    "s_waitcnt vmcnt(16)\n"
    "s_add_u32 s81, s59, 4096\n"
    "buffer_load_dwordx4 v225, s[16:19], s63 offen offset:2048 lds\n"
    "v_mfma_f32_16x16x32_bf16 a[96:99], v[176:179], v[184:187], 0\n"
    "s_add_u32 s15, s15, s77\n"
    "buffer_store_dword v249, v240, s[28:31], s81 offen offset:0 sc1\n"
    "v_mfma_f32_16x16x32_bf16 a[100:103], v[176:179], v[196:199], 0\n"
    "s_cmp_eq_u32 s57, 5\n"
    "s_cselect_b32 s15, 2147418112, s15\n"
    "s_sub_u32 s20, s57, 7\n"
    "v_mfma_f32_16x16x32_bf16 a[104:107], v[176:179], v[208:211], 0\n"
    "s_waitcnt lgkmcnt(4)\n"
    "s_and_b32 s20, s20, 7\n"
    "s_add_u32 s20, s20, 64\n"
    "v_mfma_f32_16x16x32_bf16 v[4:7], a[132:135], a[108:111], v[4:7]\n"
    "s_lshl_b32 s20, s20, 10\n"
    "ds_read_b64_tr_b16 a[120:121], v234 offset:96\n"
    "v_mfma_f32_16x16x32_bf16 v[8:11], a[136:139], a[108:111], v[8:11]\n"
    "s_add_u32 s20, s20, s77\n"
    "ds_read_b64_tr_b16 a[122:123], v234 offset:1376\n"
    "v_mfma_f32_16x16x32_bf16 v[12:15], a[140:143], a[108:111], v[12:15]\n"
    "s_waitcnt lgkmcnt(4)\n"
    "s_cmp_eq_u32 s57, 6\n"
    "s_cselect_b32 s20, 2147418112, s20\n"
    "v_mfma_f32_16x16x32_bf16 v[16:19], a[132:135], a[112:115], v[16:19]\n"
    "s_add_u32 s63, s68, s98\n"
    "s_add_u32 m0, s60, 12288\n"
    "ds_read_b64_tr_b16 a[124:125], v233 offset:128\n"
    "v_mfma_f32_16x16x32_bf16 v[20:23], a[136:139], a[112:115], v[20:23]\n"
    "s_sub_u32 s21, s57, 8\n"
    "ds_read_b64_tr_b16 a[126:127], v233 offset:1408\n"
    "buffer_load_dwordx4 v220, s[16:19], s63 offen offset:0 lds\n"
    "v_mfma_f32_16x16x32_bf16 v[24:27], a[140:143], a[112:115], v[24:27]\n"
    "s_waitcnt lgkmcnt(4)\n"
    "s_and_b32 s21, s21, 7\n"
    "s_add_u32 s21, s21, 72\n"
    "v_mfma_f32_16x16x32_bf16 v[28:31], a[132:135], a[116:119], v[28:31]\n"
    "s_waitcnt vmcnt(13)\n"
    "ds_read_b64_tr_b16 a[128:129], v234 offset:160\n"
    "ds_read_b128 a[204:207], v231 offset:0\n"
    "v_mfma_f32_16x16x32_bf16 v[32:35], a[136:139], a[116:119], v[32:35]\n"
    "s_lshl_b32 s21, s21, 10\n"
    "ds_read_b64_tr_b16 a[130:131], v234 offset:1440\n"
    "buffer_load_dwordx4 v221, s[16:19], s63 offen offset:1024 lds\n"
    "v_mfma_f32_16x16x32_bf16 v[36:39], a[140:143], a[116:119], v[36:39]\n"
    "s_waitcnt lgkmcnt(5)\n"
    "s_add_u32 s21, s21, s77\n"
    "ds_read_b128 a[208:211], v231 offset:2048\n"
    "v_mfma_f32_16x16x32_bf16 v[40:43], a[132:135], a[120:123], v[40:43]\n"
    "s_cmp_eq_u32 s57, 7\n"
    "s_cselect_b32 s21, 2147418112, s21\n"
    "ds_read_b64_tr_b16 a[108:109], v233 offset:192\n"
    "v_mfma_f32_16x16x32_bf16 v[44:47], a[136:139], a[120:123], v[44:47]\n"
    "s_sub_u32 s22, s57, 1\n"
    "ds_read_b64_tr_b16 a[110:111], v233 offset:1472\n"
    "ds_read_b128 a[212:215], v231 offset:4096\n"
    "v_mfma_f32_16x16x32_bf16 v[48:51], a[140:143], a[120:123], v[48:51]\n"
    "s_waitcnt lgkmcnt(7)\n"
    "s_and_b32 s22, s22, 7\n"
    "buffer_load_dwordx4 v222, s[16:19], s63 offen offset:2048 lds\n"
    "v_mfma_f32_16x16x32_bf16 v[52:55], a[132:135], a[124:127], v[52:55]\n"
    "s_add_u32 s22, s22, 80\n"
    "ds_read_b64_tr_b16 a[112:113], v234 offset:224\n"
    "ds_read_b128 a[216:219], v232 offset:0\n"
    "v_mfma_f32_16x16x32_bf16 v[56:59], a[136:139], a[124:127], v[56:59]\n"
    "s_add_u32 m0, s60, 15360\n"
    "s_lshl_b32 s22, s22, 10\n"
    "ds_read_b64_tr_b16 a[114:115], v234 offset:1504\n"
    "v_mfma_f32_16x16x32_bf16 v[60:63], a[140:143], a[124:127], v[60:63]\n"
    "s_waitcnt lgkmcnt(7)\n"
    "s_add_u32 s22, s22, s77\n"
    "ds_read_b128 a[220:223], v232 offset:2048\n"
    "v_mfma_f32_16x16x32_bf16 v[64:67], a[132:135], a[128:131], v[64:67]\n"
    "ds_read_b64_tr_b16 a[116:117], v233 offset:256\n"
    "buffer_load_dwordx4 v223, s[16:19], s63 offen offset:0 lds\n"
    "v_mfma_f32_16x16x32_bf16 v[68:71], a[136:139], a[128:131], v[68:71]\n"
    "ds_read_b64_tr_b16 a[118:119], v233 offset:1536\n"
    "ds_read_b128 a[224:227], v232 offset:4096\n"
    "v_mfma_f32_16x16x32_bf16 v[72:75], a[140:143], a[128:131], v[72:75]\n"
    "s_waitcnt lgkmcnt(8)\n"
    "s_cmp_eq_u32 s57, 0\n"
    "s_cselect_b32 s22, 2147418112, s22\n"
    "v_mfma_f32_16x16x32_bf16 v[76:79], a[132:135], a[108:111], v[76:79]\n"
    "s_sub_u32 s23, s57, 2\n"
    "s_and_b32 s23, s23, 7\n"
    "ds_read_b64_tr_b16 a[120:121], v233 offset:10240\n"
    "v_mfma_f32_16x16x32_bf16 v[80:83], a[136:139], a[108:111], v[80:83]\n"
    "s_add_u32 s23, s23, 88\n"
    "ds_read_b64_tr_b16 a[122:123], v233 offset:11520\n"
    "buffer_load_dwordx4 v224, s[16:19], s63 offen offset:1024 lds\n"
    "v_mfma_f32_16x16x32_bf16 v[84:87], a[140:143], a[108:111], v[84:87]\n"
    "s_waitcnt lgkmcnt(6)\n"
    "s_lshl_b32 s23, s23, 10\n"
    "s_add_u32 s23, s23, s77\n"
    "v_mfma_f32_16x16x32_bf16 v[88:91], a[132:135], a[112:115], v[88:91]\n"
    "s_cmp_eq_u32 s57, 1\n"
    "s_cselect_b32 s23, 2147418112, s23\n"
    "ds_read_b64_tr_b16 a[124:125], v234 offset:10272\n"
    "v_mfma_f32_16x16x32_bf16 v[92:95], a[136:139], a[112:115], v[92:95]\n"
    "s_sub_u32 s36, s57, 3\n"
    "s_and_b32 s36, s36, 7\n"
    "ds_read_b64_tr_b16 a[126:127], v234 offset:11552\n"
    "v_mfma_f32_16x16x32_bf16 v[96:99], a[140:143], a[112:115], v[96:99]\n"
    "s_waitcnt lgkmcnt(5)\n"
    "s_add_u32 s36, s36, 96\n"
    "buffer_load_dwordx4 v225, s[16:19], s63 offen offset:2048 lds\n"
    "v_mfma_f32_16x16x32_bf16 v[100:103], a[132:135], a[116:119], v[100:103]\n"
    "s_lshl_b32 s36, s36, 10\n"
    "s_add_u32 s36, s36, s77\n"
    "ds_read_b64_tr_b16 a[128:129], v233 offset:10304\n"
    "v_mfma_f32_16x16x32_bf16 v[104:107], a[136:139], a[116:119], v[104:107]\n"
    "s_cmp_eq_u32 s57, 2\n"
    "s_cselect_b32 s36, 2147418112, s36\n"
    "ds_read_b64_tr_b16 a[130:131], v233 offset:11584\n"
    "v_mfma_f32_16x16x32_bf16 v[108:111], a[140:143], a[116:119], v[108:111]\n"
    "s_waitcnt lgkmcnt(4)\n"
    "s_sub_u32 s37, s57, 4\n"
    "s_and_b32 s37, s37, 7\n"
    "v_mfma_f32_16x16x32_bf16 v[4:7], a[144:147], a[120:123], v[4:7]\n"
    "s_waitcnt vmcnt(7)\n"
    "ds_read_b64_tr_b16 a[108:109], v234 offset:10336\n"
    "v_mfma_f32_16x16x32_bf16 v[8:11], a[148:151], a[120:123], v[8:11]\n"
    "ds_read_b64_tr_b16 a[110:111], v234 offset:11616\n"
    "ds_read_b128 a[228:231], v231 offset:6144\n"
    "v_mfma_f32_16x16x32_bf16 v[12:15], a[152:155], a[120:123], v[12:15]\n"
    "s_waitcnt lgkmcnt(5)\n"
    "s_add_u32 s37, s37, 104\n"
    "ds_read_b128 a[232:235], v231 offset:8192\n"
    "v_mfma_f32_16x16x32_bf16 v[16:19], a[144:147], a[124:127], v[16:19]\n"
    "s_lshl_b32 s37, s37, 10\n"
    "s_add_u32 s37, s37, s77\n"
    "ds_read_b64_tr_b16 a[112:113], v233 offset:10368\n"
    "v_mfma_f32_16x16x32_bf16 v[20:23], a[148:151], a[124:127], v[20:23]\n"
    "ds_read_b64_tr_b16 a[114:115], v233 offset:11648\n"
    "ds_read_b128 a[236:239], v231 offset:10240\n"
    "v_mfma_f32_16x16x32_bf16 v[24:27], a[152:155], a[124:127], v[24:27]\n"
    "s_waitcnt lgkmcnt(7)\n"
    "v_cvt_pk_bf16_f32 v118, v4, v5\n"
    "v_cvt_pk_bf16_f32 v119, v6, v7\n"
    "v_mfma_f32_16x16x32_bf16 v[28:31], a[144:147], a[128:131], v[28:31]\n"
    "ds_read_b64_tr_b16 a[116:117], v234 offset:10400\n"
    "ds_read_b128 a[240:243], v232 offset:6144\n"
    "v_mfma_f32_16x16x32_bf16 v[32:35], a[148:151], a[128:131], v[32:35]\n"
    "ds_read_b64_tr_b16 a[118:119], v234 offset:11680\n"
    "v_cvt_pk_bf16_f32 v112, v8, v9\n"
    "v_mfma_f32_16x16x32_bf16 v[36:39], a[152:155], a[128:131], v[36:39]\n"
    "s_waitcnt lgkmcnt(8)\n"
    "ds_read_b128 a[244:247], v232 offset:8192\n"
    "v_cvt_pk_bf16_f32 v113, v10, v11\n"
    "v_mfma_f32_16x16x32_bf16 v[40:43], a[144:147], a[108:111], v[40:43]\n"
    "ds_read_b64_tr_b16 a[120:121], v233 offset:10432\n"
    "v_cvt_pk_bf16_f32 v114, v12, v13\n"
    "v_mfma_f32_16x16x32_bf16 v[44:47], a[148:151], a[108:111], v[44:47]\n"
    "ds_read_b64_tr_b16 a[122:123], v233 offset:11712\n"
    "ds_read_b128 a[248:251], v232 offset:10240\n"
    "v_mfma_f32_16x16x32_bf16 v[48:51], a[152:155], a[108:111], v[48:51]\n"
    "s_waitcnt lgkmcnt(8)\n"
    "v_cvt_pk_bf16_f32 v115, v14, v15\n"
    "v_cvt_pk_bf16_f32 v126, v16, v17\n"
    "v_mfma_f32_16x16x32_bf16 v[52:55], a[144:147], a[112:115], v[52:55]\n"
    "ds_read_b64_tr_b16 a[124:125], v234 offset:10464\n"
    "v_cvt_pk_bf16_f32 v127, v18, v19\n"
    "v_mfma_f32_16x16x32_bf16 v[56:59], a[148:151], a[112:115], v[56:59]\n"
    "ds_read_b64_tr_b16 a[126:127], v234 offset:11744\n"
    "v_cvt_pk_bf16_f32 v120, v20, v21\n"
    "v_mfma_f32_16x16x32_bf16 v[60:63], a[152:155], a[112:115], v[60:63]\n"
    "s_waitcnt lgkmcnt(6)\n"
    "buffer_load_dword v248, v241, s[28:31], 0 offen offset:0 sc1\n"
    "v_cvt_pk_bf16_f32 v121, v22, v23\n"
    "v_mfma_f32_16x16x32_bf16 v[64:67], a[144:147], a[116:119], v[64:67]\n"
    "ds_read_b64_tr_b16 a[128:129], v233 offset:10496\n"
    "v_cvt_pk_bf16_f32 v122, v24, v25\n"
    "v_mfma_f32_16x16x32_bf16 v[68:71], a[148:151], a[116:119], v[68:71]\n"
    "ds_read_b64_tr_b16 a[130:131], v233 offset:11776\n"
    "s_barrier\n"
    "v_mfma_f32_16x16x32_bf16 v[72:75], a[152:155], a[116:119], v[72:75]\n"
    "s_waitcnt lgkmcnt(5)\n"
    "v_cvt_pk_bf16_f32 v123, v26, v27\n"
    "v_cvt_pk_bf16_f32 v134, v28, v29\n"
    "v_mfma_f32_16x16x32_bf16 v[76:79], a[144:147], a[120:123], v[76:79]\n"
    "s_add_u32 s64, s72, s46\n"
    "ds_read_b64_tr_b16 a[108:109], v235 offset:0\n"
    "v_cvt_pk_bf16_f32 v135, v30, v31\n"
    "v_mfma_f32_16x16x32_bf16 v[80:83], a[148:151], a[120:123], v[80:83]\n"
    "s_add_u32 m0, s50, 40960\n"
    "ds_read_b64_tr_b16 a[110:111], v235 offset:1280\n"
    "buffer_load_dwordx4 v226, s[24:27], s64 offen offset:0 lds\n"
    "v_mfma_f32_16x16x32_bf16 v[84:87], a[152:155], a[120:123], v[84:87]\n"
    "s_waitcnt lgkmcnt(4)\n"
    "buffer_load_dwordx4 v227, s[24:27], s64 offen offset:1024 lds\n"
    "v_cvt_pk_bf16_f32 v128, v32, v33\n"
    "v_mfma_f32_16x16x32_bf16 v[88:91], a[144:147], a[124:127], v[88:91]\n"
    "ds_read_b64_tr_b16 a[112:113], v235 offset:10240\n"
    "v_cvt_pk_bf16_f32 v129, v34, v35\n"
    "v_mfma_f32_16x16x32_bf16 v[92:95], a[148:151], a[124:127], v[92:95]\n"
    "ds_read_b64_tr_b16 a[114:115], v235 offset:11520\n"
    "buffer_load_dwordx4 v228, s[24:27], s64 offen offset:2048 lds\n"
    "v_mfma_f32_16x16x32_bf16 v[96:99], a[152:155], a[124:127], v[96:99]\n"
    "s_waitcnt lgkmcnt(4)\n"
    "v_cvt_pk_bf16_f32 v130, v36, v37\n"
    "v_cvt_pk_bf16_f32 v131, v38, v39\n"
    "v_mfma_f32_16x16x32_bf16 v[100:103], a[144:147], a[128:131], v[100:103]\n"
    "s_add_u32 m0, s50, 44032\n"
    "ds_read_b64_tr_b16 a[116:117], v233 offset:20480\n"
    "buffer_load_dwordx4 v229, s[24:27], s64 offen offset:0 lds\n"
    "v_mfma_f32_16x16x32_bf16 v[104:107], a[148:151], a[128:131], v[104:107]\n"
    "ds_read_b64_tr_b16 a[118:119], v233 offset:21760\n"
    "v_cvt_pk_bf16_f32 v142, v40, v41\n"
    "v_mfma_f32_16x16x32_bf16 v[108:111], a[152:155], a[128:131], v[108:111]\n"
    "s_waitcnt lgkmcnt(4)\n"
    "v_cvt_pk_bf16_f32 v143, v42, v43\n"
    "v_cvt_pk_bf16_f32 v136, v44, v45\n"
    "v_mfma_f32_16x16x32_bf16 v[4:7], a[204:207], a[108:111], 0\n"
    "ds_read_b64_tr_b16 a[120:121], v233 offset:30720\n"
    "buffer_load_dwordx4 v230, s[24:27], s64 offen offset:1024 lds\n"
    "v_mfma_f32_16x16x32_bf16 v[8:11], a[208:211], a[108:111], 0\n"
    "ds_read_b64_tr_b16 a[122:123], v233 offset:32000\n"
    "v_cvt_pk_bf16_f32 v137, v46, v47\n"
    "v_mfma_f32_16x16x32_bf16 v[12:15], a[212:215], a[108:111], 0\n"
    "s_waitcnt lgkmcnt(4)\n"
    "s_barrier\n"
    "v_cvt_pk_bf16_f32 v138, v48, v49\n"
    "v_mfma_f32_16x16x32_bf16 v[4:7], a[216:219], a[112:115], v[4:7]\n"
    "ds_read_b64_tr_b16 a[124:125], v236 offset:32\n"
    "v_cvt_pk_bf16_f32 v139, v50, v51\n"
    "v_mfma_f32_16x16x32_bf16 v[8:11], a[220:223], a[112:115], v[8:11]\n"
    "ds_read_b64_tr_b16 a[126:127], v236 offset:1312\n"
    "v_cvt_pk_bf16_f32 v150, v52, v53\n"
    "v_mfma_f32_16x16x32_bf16 v[12:15], a[224:227], a[112:115], v[12:15]\n"
    "s_waitcnt lgkmcnt(4)\n"
    "v_cvt_pk_bf16_f32 v151, v54, v55\n"
    "v_cvt_pk_bf16_f32 v144, v56, v57\n"
    "v_mfma_f32_16x16x32_bf16 v[4:7], a[228:231], a[116:119], v[4:7]\n"
    "ds_read_b64_tr_b16 a[128:129], v236 offset:10272\n"
    "v_cvt_pk_bf16_f32 v145, v58, v59\n"
    "v_mfma_f32_16x16x32_bf16 v[8:11], a[232:235], a[116:119], v[8:11]\n"
    "ds_read_b64_tr_b16 a[130:131], v236 offset:11552\n"
    "v_cvt_pk_bf16_f32 v146, v60, v61\n"
    "v_mfma_f32_16x16x32_bf16 v[12:15], a[236:239], a[116:119], v[12:15]\n"
    "s_waitcnt lgkmcnt(4)\n"
    "v_cvt_pk_bf16_f32 v147, v62, v63\n"
    "v_cvt_pk_bf16_f32 v158, v64, v65\n"
    "v_mfma_f32_16x16x32_bf16 v[4:7], a[240:243], a[120:123], v[4:7]\n"
    "ds_read_b64_tr_b16 a[108:109], v234 offset:20512\n"
    "v_cvt_pk_bf16_f32 v159, v66, v67\n"
    "v_cvt_pk_bf16_f32 v152, v68, v69\n"
    "v_cvt_pk_bf16_f32 v153, v70, v71\n"
    "v_cvt_pk_bf16_f32 v154, v72, v73\n"
    "v_cvt_pk_bf16_f32 v155, v74, v75\n"
    "v_cvt_pk_bf16_f32 v166, v76, v77\n"
    "v_cvt_pk_bf16_f32 v167, v78, v79\n"
    "v_cvt_pk_bf16_f32 v160, v80, v81\n"
    "v_cvt_pk_bf16_f32 v161, v82, v83\n"
    "v_cvt_pk_bf16_f32 v162, v84, v85\n"
    "v_cvt_pk_bf16_f32 v163, v86, v87\n"
    "v_mfma_f32_16x16x32_bf16 v[8:11], a[244:247], a[120:123], v[8:11]\n"
    "ds_read_b64_tr_b16 a[110:111], v234 offset:21792\n"
    "v_cvt_pk_bf16_f32 v174, v88, v89\n"
    "v_mfma_f32_16x16x32_bf16 v[12:15], a[248:251], a[120:123], v[12:15]\n"
    "s_waitcnt lgkmcnt(4)\n"
    "v_cvt_pk_bf16_f32 v175, v90, v91\n"
    "v_cvt_pk_bf16_f32 v168, v92, v93\n"
    "v_mfma_f32_16x16x32_bf16 v[16:19], a[204:207], a[124:127], 0\n"
    "s_add_u32 s64, s72, s47\n"
    "ds_read_b64_tr_b16 a[112:113], v234 offset:30752\n"
    "v_cvt_pk_bf16_f32 v169, v94, v95\n"
    "v_mfma_f32_16x16x32_bf16 v[20:23], a[208:211], a[124:127], 0\n"
    "s_add_u32 m0, s50, 0\n"
    "ds_read_b64_tr_b16 a[114:115], v234 offset:32032\n"
    "buffer_load_dwordx4 v226, s[24:27], s64 offen offset:0 lds\n"
    "v_mfma_f32_16x16x32_bf16 v[24:27], a[212:215], a[124:127], 0\n"
    "s_waitcnt lgkmcnt(4)\n"
    "buffer_load_dwordx4 v227, s[24:27], s64 offen offset:1024 lds\n"
    "v_cvt_pk_bf16_f32 v170, v96, v97\n"
    "v_mfma_f32_16x16x32_bf16 v[16:19], a[216:219], a[128:131], v[16:19]\n"
    "ds_read_b64_tr_b16 a[116:117], v235 offset:64\n"
    "buffer_load_dwordx4 v228, s[24:27], s64 offen offset:2048 lds\n"
    "v_mfma_f32_16x16x32_bf16 v[20:23], a[220:223], a[128:131], v[20:23]\n"
    "s_add_u32 m0, s50, 3072\n"
    "ds_read_b64_tr_b16 a[118:119], v235 offset:1344\n"
    "buffer_load_dwordx4 v229, s[24:27], s64 offen offset:0 lds\n"
    "v_mfma_f32_16x16x32_bf16 v[24:27], a[224:227], a[128:131], v[24:27]\n"
    "s_waitcnt lgkmcnt(4)\n"
    "buffer_load_dwordx4 v230, s[24:27], s64 offen offset:1024 lds\n"
    "v_cvt_pk_bf16_f32 v171, v98, v99\n"
    "v_mfma_f32_16x16x32_bf16 v[16:19], a[228:231], a[108:111], v[16:19]\n"
    "s_add_u32 s63, s68, s99\n"
    "ds_read_b64_tr_b16 a[120:121], v235 offset:10304\n"
    "v_cvt_pk_bf16_f32 v182, v100, v101\n"
    "v_mfma_f32_16x16x32_bf16 v[20:23], a[232:235], a[108:111], v[20:23]\n"
    "s_add_u32 m0, s60, 0\n"
    "ds_read_b64_tr_b16 a[122:123], v235 offset:11584\n"
    "buffer_load_dwordx4 v220, s[16:19], s63 offen offset:0 lds\n"
    "v_mfma_f32_16x16x32_bf16 v[24:27], a[236:239], a[108:111], v[24:27]\n"
    "s_waitcnt lgkmcnt(4)\n"
    "s_waitcnt vmcnt(11)\n"
    "v_cmp_ne_u32 vcc, v249, v248\n"
    "v_cvt_pk_bf16_f32 v183, v102, v103\n"
    "v_mfma_f32_16x16x32_bf16 v[16:19], a[240:243], a[112:115], v[16:19]\n"
    "s_nop 1\n"
    "s_cbranch_vccnz L_f1_slow_%=\n"
    "L_f1_ret_%=:\n"
    "ds_read_b64_tr_b16 a[124:125], v233 offset:20544\n"
    "buffer_load_dwordx4 v221, s[16:19], s63 offen offset:1024 lds\n"
    "v_cvt_pk_bf16_f32 v176, v104, v105\n"
    "v_cvt_pk_bf16_f32 v177, v106, v107\n"
    "v_cvt_pk_bf16_f32 v178, v108, v109\n"
    "v_cvt_pk_bf16_f32 v179, v110, v111\n"
    "v_mfma_f32_16x16x32_bf16 v[20:23], a[244:247], a[112:115], v[20:23]\n"
    "ds_read_b64_tr_b16 a[126:127], v233 offset:21824\n"
    "buffer_load_dwordx4 v[190:193], v237, s[28:31], s74 offen offset:0 sc1\n"
    "v_cvt_pk_bf16_f32 v28, v4, v5\n"
    "v_cvt_pk_bf16_f32 v29, v6, v7\n"
    "v_cvt_pk_bf16_f32 v30, v8, v9\n"
    "v_cvt_pk_bf16_f32 v31, v10, v11\n"
    "v_cvt_pk_bf16_f32 v32, v12, v13\n"
    "v_cvt_pk_bf16_f32 v33, v14, v15\n"
    "v_mfma_f32_16x16x32_bf16 v[24:27], a[248:251], a[112:115], v[24:27]\n"
    "s_waitcnt lgkmcnt(4)\n"
    "buffer_load_dwordx2 v[194:195], v238, s[28:31], s74 offen offset:0 sc1\n"
    "v_mfma_f32_16x16x32_bf16 v[4:7], a[204:207], a[116:119], 0\n"
    "ds_read_b64_tr_b16 a[128:129], v233 offset:30784\n"
    "buffer_load_dwordx4 v222, s[16:19], s63 offen offset:2048 lds\n"
    "v_mfma_f32_16x16x32_bf16 v[8:11], a[208:211], a[116:119], 0\n"
    "s_add_u32 m0, s60, 3072\n"
    "ds_read_b64_tr_b16 a[130:131], v233 offset:32064\n"
    "buffer_load_dwordx4 v[202:205], v237, s[28:31], s74 offen offset:1024 sc1\n"
    "v_mfma_f32_16x16x32_bf16 v[12:15], a[212:215], a[116:119], 0\n"
    "s_waitcnt lgkmcnt(4)\n"
    "buffer_load_dwordx2 v[206:207], v238, s[28:31], s74 offen offset:512 sc1\n"
    "v_cvt_pk_bf16_f32 v36, v16, v17\n"
    "v_mfma_f32_16x16x32_bf16 v[4:7], a[216:219], a[120:123], v[4:7]\n"
    "ds_read_b64_tr_b16 a[108:109], v236 offset:96\n"
    "buffer_load_dwordx4 v223, s[16:19], s63 offen offset:0 lds\n"
    "v_mfma_f32_16x16x32_bf16 v[8:11], a[220:223], a[120:123], v[8:11]\n"
    "ds_read_b64_tr_b16 a[110:111], v236 offset:1376\n"
    "buffer_load_dwordx4 v[214:217], v237, s[28:31], s74 offen offset:2048 sc1\n"
    "v_mfma_f32_16x16x32_bf16 v[12:15], a[224:227], a[120:123], v[12:15]\n"
    "s_waitcnt lgkmcnt(4)\n"
    "buffer_load_dwordx2 v[218:219], v238, s[28:31], s74 offen offset:1024 sc1\n"
    "v_cvt_pk_bf16_f32 v37, v18, v19\n"
    "v_mfma_f32_16x16x32_bf16 v[4:7], a[228:231], a[124:127], v[4:7]\n"
    "ds_read_b64_tr_b16 a[112:113], v236 offset:10336\n"
    "buffer_load_dwordx4 v224, s[16:19], s63 offen offset:1024 lds\n"
    "v_mfma_f32_16x16x32_bf16 v[8:11], a[232:235], a[124:127], v[8:11]\n"
    "ds_read_b64_tr_b16 a[114:115], v236 offset:11616\n"
    "v_cvt_pk_bf16_f32 v38, v20, v21\n"
    "v_mfma_f32_16x16x32_bf16 v[12:15], a[236:239], a[124:127], v[12:15]\n"
    "s_waitcnt lgkmcnt(4)\n"
    "v_cvt_pk_bf16_f32 v39, v22, v23\n"
    "v_cvt_pk_bf16_f32 v40, v24, v25\n"
    "v_mfma_f32_16x16x32_bf16 v[4:7], a[240:243], a[128:131], v[4:7]\n"
    "ds_read_b64_tr_b16 a[116:117], v234 offset:20576\n"
    "buffer_load_dwordx4 v225, s[16:19], s63 offen offset:2048 lds\n"
    "v_mfma_f32_16x16x32_bf16 v[8:11], a[244:247], a[128:131], v[8:11]\n"
    "ds_read_b64_tr_b16 a[118:119], v234 offset:21856\n"
    "v_cvt_pk_bf16_f32 v41, v26, v27\n"
    "v_mfma_f32_16x16x32_bf16 v[12:15], a[248:251], a[128:131], v[12:15]\n"
    "s_waitcnt lgkmcnt(4)\n"
    "s_cmp_eq_u32 s57, 3\n"
    "s_cselect_b32 s37, 2147418112, s37\n"
    "v_mfma_f32_16x16x32_bf16 v[16:19], a[204:207], a[108:111], 0\n"
    "s_movk_i32 s42, 0x3f80\n"
    "s_mov_b32 s43, 0x3f800000\n"
    "ds_read_b64_tr_b16 a[120:121], v234 offset:30816\n"
    "v_mfma_f32_16x16x32_bf16 v[20:23], a[208:211], a[108:111], 0\n"
    "s_lshl1_add_u32 s88, s57, 0\n"
    "ds_read_b64_tr_b16 a[122:123], v234 offset:32096\n"
    "v_cvt_pk_bf16_f32 v44, v4, v5\n"
    "v_mfma_f32_16x16x32_bf16 v[24:27], a[212:215], a[108:111], 0\n"
    "s_waitcnt lgkmcnt(4)\n"
    "v_cvt_pk_bf16_f32 v45, v6, v7\n"
    "v_cvt_pk_bf16_f32 v46, v8, v9\n"
    "v_mfma_f32_16x16x32_bf16 v[16:19], a[216:219], a[112:115], v[16:19]\n"
    "s_mul_i32 s89, s88, 43\n"
    "v_cvt_pk_bf16_f32 v47, v10, v11\n"
    "v_cvt_pk_bf16_f32 v48, v12, v13\n"
    "v_mfma_f32_16x16x32_bf16 v[20:23], a[220:223], a[112:115], v[20:23]\n"
    "s_lshr_b32 s89, s89, 7\n"
    "s_mul_i32 s90, s89, 4294856736\n"
    "v_cvt_pk_bf16_f32 v49, v14, v15\n"
    "v_mfma_f32_16x16x32_bf16 v[24:27], a[224:227], a[112:115], v[24:27]\n"
    "s_waitcnt lgkmcnt(2)\n"
    "s_mul_i32 s38, s88, 36864\n"
    "s_add_u32 s38, s38, s90\n"
    "v_mfma_f32_16x16x32_bf16 v[16:19], a[228:231], a[116:119], v[16:19]\n"
    "s_cmp_lt_u32 s88, 12\n"
    "s_cselect_b32 s38, s38, 2147418112\n"
    "s_lshl1_add_u32 s88, s57, 1\n"
    "v_mfma_f32_16x16x32_bf16 v[20:23], a[232:235], a[116:119], v[20:23]\n"
    "s_mul_i32 s89, s88, 43\n"
    "s_lshr_b32 s89, s89, 7\n"
    "s_mul_i32 s90, s89, 4294856736\n"
    "v_mfma_f32_16x16x32_bf16 v[24:27], a[236:239], a[116:119], v[24:27]\n"
    "s_waitcnt lgkmcnt(0)\n"
    "s_mul_i32 s39, s88, 36864\n"
    "s_add_u32 s39, s39, s90\n"
    "v_mfma_f32_16x16x32_bf16 v[16:19], a[240:243], a[120:123], v[16:19]\n"
    "s_cmp_lt_u32 s88, 12\n"
    "s_cselect_b32 s39, s39, 2147418112\n"
    "v_mfma_f32_16x16x32_bf16 v[20:23], a[244:247], a[120:123], v[20:23]\n"
    "s_cmp_lt_u32 s57, 4\n"
    "s_cselect_b32 s88, 20, 4\n"
    "s_lshl1_add_u32 s88, s57, s88\n"
    "v_mfma_f32_16x16x32_bf16 v[24:27], a[248:251], a[120:123], v[24:27]\n"
    "s_waitcnt vmcnt(9)\n"
    "s_mul_i32 s89, s88, 43\n"
    "s_lshr_b32 s89, s89, 7\n"
    "v_mfma_f32_16x16x32_bf16 a[0:3], v[116:119], v[188:191], a[0:3]\n"
    "s_waitcnt vmcnt(6)\n"
    "s_mul_i32 s90, s89, 4294856736\n"
    "buffer_load_dwordx4 v[184:187], v237, s[28:31], s75 offen offset:0 sc1\n"
    "v_mfma_f32_16x16x32_bf16 a[4:7], v[116:119], v[200:203], a[4:7]\n"
    "s_waitcnt vmcnt(4)\n"
    "buffer_load_dwordx2 v[60:61], v238, s[28:31], s75 offen offset:0 sc1\n"
    "v_cvt_pk_bf16_f32 v52, v16, v17\n"
    "v_mfma_f32_16x16x32_bf16 a[8:11], v[116:119], v[212:215], a[8:11]\n"
    "buffer_load_dwordx4 v[62:65], v237, s[28:31], s76 offen offset:0 sc1\n"
    "v_cvt_pk_bf16_f32 v53, v18, v19\n"
    "v_mfma_f32_16x16x32_bf16 a[0:3], v[112:115], v[192:195], a[0:3]\n"
    "buffer_load_dwordx2 v[66:67], v238, s[28:31], s76 offen offset:0 sc1\n"
    "v_cvt_pk_bf16_f32 v54, v20, v21\n"
    "v_mfma_f32_16x16x32_bf16 a[4:7], v[112:115], v[204:207], a[4:7]\n"
    "s_waitcnt vmcnt(6)\n"
    "buffer_load_dwordx4 v[196:199], v237, s[28:31], s75 offen offset:1024 sc1\n"
    "v_cvt_pk_bf16_f32 v55, v22, v23\n"
    "v_mfma_f32_16x16x32_bf16 a[8:11], v[112:115], v[216:219], a[8:11]\n"
    "ds_read_b128 a[156:159], v231 offset:12288\n"
    "buffer_load_dwordx2 v[68:69], v238, s[28:31], s75 offen offset:512 sc1\n"
    "v_mfma_f32_16x16x32_bf16 a[12:15], v[124:127], v[188:191], a[12:15]\n"
    "s_mul_i32 s40, s88, 36864\n"
    "ds_read_b128 a[160:163], v231 offset:14336\n"
    "buffer_load_dwordx4 v[70:73], v237, s[28:31], s76 offen offset:1024 sc1\n"
    "v_mfma_f32_16x16x32_bf16 a[16:19], v[124:127], v[200:203], a[16:19]\n"
    "s_add_u32 s40, s40, s90\n"
    "buffer_load_dwordx2 v[74:75], v238, s[28:31], s76 offen offset:512 sc1\n"
    "v_cvt_pk_bf16_f32 v56, v24, v25\n"
    "v_mfma_f32_16x16x32_bf16 a[20:23], v[124:127], v[212:215], a[20:23]\n"
    "ds_read_b128 a[164:167], v231 offset:16384\n"
    "buffer_load_dwordx4 v[208:211], v237, s[28:31], s75 offen offset:2048 sc1\n"
    "v_mfma_f32_16x16x32_bf16 a[12:15], v[120:123], v[192:195], a[12:15]\n"
    "buffer_load_dwordx2 v[76:77], v238, s[28:31], s75 offen offset:1024 sc1\n"
    "v_cvt_pk_bf16_f32 v57, v26, v27\n"
    "v_mfma_f32_16x16x32_bf16 a[16:19], v[120:123], v[204:207], a[16:19]\n"
    "ds_read_b128 a[168:171], v232 offset:12288\n"
    "buffer_load_dwordx4 v[78:81], v237, s[28:31], s76 offen offset:2048 sc1\n"
    "v_mfma_f32_16x16x32_bf16 a[20:23], v[120:123], v[216:219], a[20:23]\n"
    "s_cmp_lt_u32 s88, 27\n"
    "s_cselect_b32 s40, s40, 2147418112\n"
    "buffer_load_dwordx2 v[82:83], v238, s[28:31], s76 offen offset:1024 sc1\n"
    "v_mfma_f32_16x16x32_bf16 a[24:27], v[132:135], v[188:191], a[24:27]\n"
    "s_cmp_lt_u32 s57, 4\n"
    "s_cselect_b32 s88, 21, 5\n"
    "ds_read_b128 a[172:175], v232 offset:14336\n"
    "v_mfma_f32_16x16x32_bf16 a[28:31], v[132:135], v[200:203], a[28:31]\n"
    "s_lshl1_add_u32 s88, s57, s88\n"
    "s_mul_i32 s89, s88, 43\n"
    "s_lshr_b32 s89, s89, 7\n"
    "v_mfma_f32_16x16x32_bf16 a[32:35], v[132:135], v[212:215], a[32:35]\n"
    "s_mul_i32 s90, s89, 4294856736\n"
    "s_mul_i32 s41, s88, 36864\n"
    "ds_read_b128 a[176:179], v232 offset:16384\n"
    "v_mfma_f32_16x16x32_bf16 a[24:27], v[128:131], v[192:195], a[24:27]\n"
    "s_add_u32 s41, s41, s90\n"
    "s_cmp_lt_u32 s88, 27\n"
    "s_cselect_b32 s41, s41, 2147418112\n"
    "v_mfma_f32_16x16x32_bf16 a[28:31], v[128:131], v[204:207], a[28:31]\n"
    "v_mfma_f32_16x16x32_bf16 a[32:35], v[128:131], v[216:219], a[32:35]\n"
    "v_mfma_f32_16x16x32_bf16 a[36:39], v[140:143], v[188:191], a[36:39]\n"
    "v_mfma_f32_16x16x32_bf16 a[40:43], v[140:143], v[200:203], a[40:43]\n"
    "v_mfma_f32_16x16x32_bf16 a[44:47], v[140:143], v[212:215], a[44:47]\n"
    "v_mfma_f32_16x16x32_bf16 a[36:39], v[136:139], v[192:195], a[36:39]\n"
    "v_mfma_f32_16x16x32_bf16 a[40:43], v[136:139], v[204:207], a[40:43]\n"
    "v_mfma_f32_16x16x32_bf16 a[44:47], v[136:139], v[216:219], a[44:47]\n"
    "v_mfma_f32_16x16x32_bf16 a[48:51], v[148:151], v[188:191], a[48:51]\n"
    "s_waitcnt lgkmcnt(0)\n"
    "v_mfma_f32_16x16x32_bf16 a[52:55], v[148:151], v[200:203], a[52:55]\n"
    "v_mfma_f32_16x16x32_bf16 a[56:59], v[148:151], v[212:215], a[56:59]\n"
    "v_mfma_f32_16x16x32_bf16 a[48:51], v[144:147], v[192:195], a[48:51]\n"
    "v_mfma_f32_16x16x32_bf16 a[52:55], v[144:147], v[204:207], a[52:55]\n"
    "v_mfma_f32_16x16x32_bf16 a[56:59], v[144:147], v[216:219], a[56:59]\n"
    "s_waitcnt vmcnt(12)\n"
    "ds_read_b128 a[180:183], v231 offset:0\n"
    "v_mfma_f32_16x16x32_bf16 a[60:63], v[156:159], v[188:191], a[60:63]\n"
    "v_mfma_f32_16x16x32_bf16 a[64:67], v[156:159], v[200:203], a[64:67]\n"
    "ds_read_b128 a[184:187], v231 offset:2048\n"
    "v_mfma_f32_16x16x32_bf16 a[68:71], v[156:159], v[212:215], a[68:71]\n"
    "v_mfma_f32_16x16x32_bf16 a[60:63], v[152:155], v[192:195], a[60:63]\n"
    "ds_read_b128 a[188:191], v231 offset:4096\n"
    "v_mfma_f32_16x16x32_bf16 a[64:67], v[152:155], v[204:207], a[64:67]\n"
    "v_mfma_f32_16x16x32_bf16 a[68:71], v[152:155], v[216:219], a[68:71]\n"
    "ds_read_b128 a[192:195], v232 offset:0\n"
    "v_mfma_f32_16x16x32_bf16 a[72:75], v[164:167], v[188:191], a[72:75]\n"
    "v_mfma_f32_16x16x32_bf16 a[76:79], v[164:167], v[200:203], a[76:79]\n"
    "ds_read_b128 a[196:199], v232 offset:2048\n"
    "v_mfma_f32_16x16x32_bf16 a[80:83], v[164:167], v[212:215], a[80:83]\n"
    "v_mfma_f32_16x16x32_bf16 a[72:75], v[160:163], v[192:195], a[72:75]\n"
    "ds_read_b128 a[200:203], v232 offset:4096\n"
    "v_mfma_f32_16x16x32_bf16 a[76:79], v[160:163], v[204:207], a[76:79]\n"
    "v_mfma_f32_16x16x32_bf16 a[80:83], v[160:163], v[216:219], a[80:83]\n"
    "v_mfma_f32_16x16x32_bf16 a[84:87], v[172:175], v[188:191], a[84:87]\n"
    "v_mfma_f32_16x16x32_bf16 a[88:91], v[172:175], v[200:203], a[88:91]\n"
    "s_barrier\n"
    "v_mfma_f32_16x16x32_bf16 a[92:95], v[172:175], v[212:215], a[92:95]\n"
    "v_mfma_f32_16x16x32_bf16 a[84:87], v[168:171], v[192:195], a[84:87]\n"
    "ds_read_b64_tr_b16 a[124:125], v233 offset:40960\n"
    "v_mfma_f32_16x16x32_bf16 a[88:91], v[168:171], v[204:207], a[88:91]\n"
    "ds_read_b64_tr_b16 a[126:127], v233 offset:42240\n"
    "v_mfma_f32_16x16x32_bf16 a[92:95], v[168:171], v[216:219], a[92:95]\n"
    "v_mfma_f32_16x16x32_bf16 a[96:99], v[180:183], v[188:191], a[96:99]\n"
    "ds_read_b64_tr_b16 a[128:129], v233 offset:51200\n"
    "v_mfma_f32_16x16x32_bf16 a[100:103], v[180:183], v[200:203], a[100:103]\n"
    "ds_read_b64_tr_b16 a[130:131], v233 offset:52480\n"
    "v_mfma_f32_16x16x32_bf16 a[104:107], v[180:183], v[212:215], a[104:107]\n"
    "v_mfma_f32_16x16x32_bf16 a[96:99], v[176:179], v[192:195], a[96:99]\n"
    "ds_read_b64_tr_b16 a[108:109], v233 offset:0\n"
    "v_mfma_f32_16x16x32_bf16 a[100:103], v[176:179], v[204:207], a[100:103]\n"
    "ds_read_b64_tr_b16 a[110:111], v233 offset:1280\n"
    "v_mfma_f32_16x16x32_bf16 a[104:107], v[176:179], v[216:219], a[104:107]\n"
    "s_waitcnt lgkmcnt(4)\n"
    "v_mfma_f32_16x16x32_bf16 v[4:7], a[156:159], a[124:127], 0\n"
    "ds_read_b64_tr_b16 a[112:113], v233 offset:10240\n"
    "v_mfma_f32_16x16x32_bf16 v[8:11], a[160:163], a[124:127], 0\n"
    "ds_read_b64_tr_b16 a[114:115], v233 offset:11520\n"
    "v_mfma_f32_16x16x32_bf16 v[12:15], a[164:167], a[124:127], 0\n"
    "s_waitcnt vmcnt(11)\n"
    "v_mfma_f32_16x16x32_bf16 a[0:3], v[28:31], v[184:187], a[0:3]\n"
    "s_waitcnt vmcnt(7)\n"
    "v_mfma_f32_16x16x32_bf16 a[4:7], v[28:31], v[196:199], a[4:7]\n"
    "s_waitcnt vmcnt(3)\n"
    "v_mfma_f32_16x16x32_bf16 a[8:11], v[28:31], v[208:211], a[8:11]\n"
    "s_waitcnt lgkmcnt(4)\n"
    "v_mfma_f32_16x16x32_bf16 v[4:7], a[168:171], a[128:131], v[4:7]\n"
    "ds_read_b64_tr_b16 a[116:117], v234 offset:40992\n"
    "v_mfma_f32_16x16x32_bf16 v[8:11], a[172:175], a[128:131], v[8:11]\n"
    "ds_read_b64_tr_b16 a[118:119], v234 offset:42272\n"
    "v_mfma_f32_16x16x32_bf16 v[12:15], a[176:179], a[128:131], v[12:15]\n"
    "s_waitcnt lgkmcnt(4)\n"
    "v_mfma_f32_16x16x32_bf16 v[4:7], a[180:183], a[108:111], v[4:7]\n"
    "ds_read_b64_tr_b16 a[120:121], v234 offset:51232\n"
    "v_mfma_f32_16x16x32_bf16 v[8:11], a[184:187], a[108:111], v[8:11]\n"
    "ds_read_b64_tr_b16 a[122:123], v234 offset:52512\n"
    "v_mfma_f32_16x16x32_bf16 v[12:15], a[188:191], a[108:111], v[12:15]\n"
    "s_waitcnt lgkmcnt(4)\n"
    "v_mfma_f32_16x16x32_bf16 v[4:7], a[192:195], a[112:115], v[4:7]\n"
    "ds_read_b64_tr_b16 a[124:125], v234 offset:32\n"
    "v_mfma_f32_16x16x32_bf16 v[8:11], a[196:199], a[112:115], v[8:11]\n"
    "ds_read_b64_tr_b16 a[126:127], v234 offset:1312\n"
    "v_mfma_f32_16x16x32_bf16 v[12:15], a[200:203], a[112:115], v[12:15]\n"
    "s_waitcnt lgkmcnt(4)\n"
    "v_mfma_f32_16x16x32_bf16 v[16:19], a[156:159], a[116:119], 0\n"
    "ds_read_b64_tr_b16 a[128:129], v234 offset:10272\n"
    "v_mfma_f32_16x16x32_bf16 v[20:23], a[160:163], a[116:119], 0\n"
    "ds_read_b64_tr_b16 a[130:131], v234 offset:11552\n"
    "v_cvt_pk_bf16_f32 v34, v4, v5\n"
    "v_mfma_f32_16x16x32_bf16 v[24:27], a[164:167], a[116:119], 0\n"
    "v_cvt_pk_bf16_f32 v35, v6, v7\n"
    "v_cvt_pk_bf16_f32 v28, v8, v9\n"
    "v_mfma_f32_16x16x32_bf16 a[12:15], v[36:39], v[184:187], a[12:15]\n"
    "v_cvt_pk_bf16_f32 v29, v10, v11\n"
    "v_cvt_pk_bf16_f32 v30, v12, v13\n"
    "v_mfma_f32_16x16x32_bf16 a[16:19], v[36:39], v[196:199], a[16:19]\n"
    "v_cvt_pk_bf16_f32 v31, v14, v15\n"
    "v_mfma_f32_16x16x32_bf16 a[20:23], v[36:39], v[208:211], a[20:23]\n"
    "s_waitcnt lgkmcnt(4)\n"
    "v_mfma_f32_16x16x32_bf16 v[16:19], a[168:171], a[120:123], v[16:19]\n"
    "ds_read_b64_tr_b16 a[108:109], v233 offset:41024\n"
    "v_mfma_f32_16x16x32_bf16 v[20:23], a[172:175], a[120:123], v[20:23]\n"
    "ds_read_b64_tr_b16 a[110:111], v233 offset:42304\n"
    "v_mfma_f32_16x16x32_bf16 v[24:27], a[176:179], a[120:123], v[24:27]\n"
    "v_mfma_f32_16x16x32_bf16 a[0:3], v[32:35], v[60:63], a[0:3]\n"
    "v_mfma_f32_16x16x32_bf16 a[4:7], v[32:35], v[68:71], a[4:7]\n"
    "s_waitcnt vmcnt(1)\n"
    "v_mfma_f32_16x16x32_bf16 a[8:11], v[32:35], v[76:79], a[8:11]\n"
    "s_waitcnt lgkmcnt(4)\n"
    "v_mfma_f32_16x16x32_bf16 v[16:19], a[180:183], a[124:127], v[16:19]\n"
    "ds_read_b64_tr_b16 a[112:113], v233 offset:51264\n"
    "v_mfma_f32_16x16x32_bf16 v[20:23], a[184:187], a[124:127], v[20:23]\n"
    "ds_read_b64_tr_b16 a[114:115], v233 offset:52544\n"
    "v_mfma_f32_16x16x32_bf16 v[24:27], a[188:191], a[124:127], v[24:27]\n"
    "v_mfma_f32_16x16x32_bf16 a[0:3], v[28:31], v[64:67], a[0:3]\n"
    "v_mfma_f32_16x16x32_bf16 a[4:7], v[28:31], v[72:75], a[4:7]\n"
    "s_waitcnt vmcnt(0)\n"
    "v_mfma_f32_16x16x32_bf16 a[8:11], v[28:31], v[80:83], a[8:11]\n"
    "s_waitcnt lgkmcnt(4)\n"
    "v_mfma_f32_16x16x32_bf16 v[16:19], a[192:195], a[128:131], v[16:19]\n"
    "ds_read_b64_tr_b16 a[116:117], v233 offset:64\n"
    "s_nop 1\n"
    "v_accvgpr_read_b32 v112, a0\n"
    "v_mfma_f32_16x16x32_bf16 v[20:23], a[196:199], a[128:131], v[20:23]\n"
    "ds_read_b64_tr_b16 a[118:119], v233 offset:1344\n"
    "v_accvgpr_read_b32 v113, a1\n"
    "v_mfma_f32_16x16x32_bf16 v[24:27], a[200:203], a[128:131], v[24:27]\n"
    "s_waitcnt lgkmcnt(4)\n"
    "v_accvgpr_read_b32 v114, a2\n"
    "v_accvgpr_read_b32 v115, a3\n"
    "v_mfma_f32_16x16x32_bf16 v[4:7], a[156:159], a[108:111], 0\n"
    "ds_read_b64_tr_b16 a[120:121], v233 offset:10304\n"
    "v_accvgpr_read_b32 v116, a4\n"
    "v_mfma_f32_16x16x32_bf16 v[8:11], a[160:163], a[108:111], 0\n"
    "ds_read_b64_tr_b16 a[122:123], v233 offset:11584\n"
    "v_cvt_pk_bf16_f32 v42, v16, v17\n"
    "v_mfma_f32_16x16x32_bf16 v[12:15], a[164:167], a[108:111], 0\n"
    "v_cvt_pk_bf16_f32 v43, v18, v19\n"
    "v_accvgpr_read_b32 v117, a5\n"
    "v_mfma_f32_16x16x32_bf16 a[24:27], v[44:47], v[184:187], a[24:27]\n"
    "v_cvt_pk_bf16_f32 v36, v20, v21\n"
    "v_accvgpr_read_b32 v118, a6\n"
    "v_mfma_f32_16x16x32_bf16 a[28:31], v[44:47], v[196:199], a[28:31]\n"
    "v_cvt_pk_bf16_f32 v37, v22, v23\n"
    "v_accvgpr_read_b32 v119, a7\n"
    "v_mfma_f32_16x16x32_bf16 a[32:35], v[44:47], v[208:211], a[32:35]\n"
    "s_waitcnt lgkmcnt(4)\n"
    "v_cvt_pk_bf16_f32 v38, v24, v25\n"
    "v_cvt_pk_bf16_f32 v120, v112, v113\n"
    "v_mfma_f32_16x16x32_bf16 v[4:7], a[168:171], a[112:115], v[4:7]\n"
    "ds_read_b64_tr_b16 a[124:125], v234 offset:41056\n"
    "v_cvt_pk_bf16_f32 v39, v26, v27\n"
    "v_mfma_f32_16x16x32_bf16 v[8:11], a[172:175], a[112:115], v[8:11]\n"
    "ds_read_b64_tr_b16 a[126:127], v234 offset:42336\n"
    "v_cvt_pk_bf16_f32 v121, v114, v115\n"
    "v_mfma_f32_16x16x32_bf16 v[12:15], a[176:179], a[112:115], v[12:15]\n"
    "v_cvt_pk_bf16_f32 v122, v116, v117\n"
    "v_cvt_pk_bf16_f32 v123, v118, v119\n"
    "v_mfma_f32_16x16x32_bf16 a[12:15], v[40:43], v[60:63], a[12:15]\n"
    "buffer_store_dwordx4 v[120:123], v237, s[28:31], s8 offen offset:0 sc1\n"
    "v_mfma_f32_16x16x32_bf16 a[16:19], v[40:43], v[68:71], a[16:19]\n"
    "v_mfma_f32_16x16x32_bf16 a[20:23], v[40:43], v[76:79], a[20:23]\n"
    "s_waitcnt lgkmcnt(4)\n"
    "v_mfma_f32_16x16x32_bf16 v[4:7], a[180:183], a[116:119], v[4:7]\n"
    "ds_read_b64_tr_b16 a[128:129], v234 offset:51296\n"
    "v_mfma_f32_16x16x32_bf16 v[8:11], a[184:187], a[116:119], v[8:11]\n"
    "ds_read_b64_tr_b16 a[130:131], v234 offset:52576\n"
    "v_mfma_f32_16x16x32_bf16 v[12:15], a[188:191], a[116:119], v[12:15]\n"
    "v_mfma_f32_16x16x32_bf16 a[12:15], v[36:39], v[64:67], a[12:15]\n"
    "v_mfma_f32_16x16x32_bf16 a[16:19], v[36:39], v[72:75], a[16:19]\n"
    "v_mfma_f32_16x16x32_bf16 a[20:23], v[36:39], v[80:83], a[20:23]\n"
    "s_waitcnt lgkmcnt(4)\n"
    "v_accvgpr_read_b32 v124, a8\n"
    "v_accvgpr_read_b32 v125, a9\n"
    "v_mfma_f32_16x16x32_bf16 v[4:7], a[192:195], a[120:123], v[4:7]\n"
    "ds_read_b64_tr_b16 a[108:109], v234 offset:96\n"
    "v_accvgpr_read_b32 v126, a10\n"
    "v_mfma_f32_16x16x32_bf16 v[8:11], a[196:199], a[120:123], v[8:11]\n"
    "ds_read_b64_tr_b16 a[110:111], v234 offset:1376\n"
    "v_accvgpr_read_b32 v127, a11\n"
    "v_mfma_f32_16x16x32_bf16 v[12:15], a[200:203], a[120:123], v[12:15]\n"
    "s_waitcnt lgkmcnt(4)\n"
    "v_accvgpr_read_b32 v128, a12\n"
    "v_accvgpr_read_b32 v129, a13\n"
    "v_mfma_f32_16x16x32_bf16 v[16:19], a[156:159], a[124:127], 0\n"
    "ds_read_b64_tr_b16 a[112:113], v234 offset:10336\n"
    "v_accvgpr_read_b32 v130, a14\n"
    "v_mfma_f32_16x16x32_bf16 v[20:23], a[160:163], a[124:127], 0\n"
    "ds_read_b64_tr_b16 a[114:115], v234 offset:11616\n"
    "v_cvt_pk_bf16_f32 v50, v4, v5\n"
    "v_mfma_f32_16x16x32_bf16 v[24:27], a[164:167], a[124:127], 0\n"
    "v_cvt_pk_bf16_f32 v51, v6, v7\n"
    "v_accvgpr_read_b32 v131, a15\n"
    "v_mfma_f32_16x16x32_bf16 a[36:39], v[52:55], v[184:187], a[36:39]\n"
    "v_cvt_pk_bf16_f32 v44, v8, v9\n"
    "v_cvt_pk_bf16_f32 v132, v124, v125\n"
    "v_mfma_f32_16x16x32_bf16 a[40:43], v[52:55], v[196:199], a[40:43]\n"
    "v_cvt_pk_bf16_f32 v45, v10, v11\n"
    "v_cvt_pk_bf16_f32 v133, v126, v127\n"
    "v_mfma_f32_16x16x32_bf16 a[44:47], v[52:55], v[208:211], a[44:47]\n"
    "s_waitcnt lgkmcnt(4)\n"
    "v_cvt_pk_bf16_f32 v46, v12, v13\n"
    "v_cvt_pk_bf16_f32 v134, v128, v129\n"
    "v_mfma_f32_16x16x32_bf16 v[16:19], a[168:171], a[128:131], v[16:19]\n"
    "ds_read_b64_tr_b16 a[116:117], v235 offset:128\n"
    "v_cvt_pk_bf16_f32 v47, v14, v15\n"
    "v_mfma_f32_16x16x32_bf16 v[20:23], a[172:175], a[128:131], v[20:23]\n"
    "ds_read_b64_tr_b16 a[118:119], v235 offset:1408\n"
    "v_cvt_pk_bf16_f32 v135, v130, v131\n"
    "v_mfma_f32_16x16x32_bf16 v[24:27], a[176:179], a[128:131], v[24:27]\n"
    "buffer_store_dwordx4 v[132:135], v237, s[28:31], s9 offen offset:0 sc1\n"
    "v_accvgpr_read_b32 v84, a16\n"
    "v_mfma_f32_16x16x32_bf16 a[24:27], v[48:51], v[60:63], a[24:27]\n"
    "v_accvgpr_read_b32 v85, a17\n"
    "v_accvgpr_read_b32 v86, a18\n"
    "v_mfma_f32_16x16x32_bf16 a[28:31], v[48:51], v[68:71], a[28:31]\n"
    "v_accvgpr_read_b32 v87, a19\n"
    "v_accvgpr_read_b32 v88, a20\n"
    "v_mfma_f32_16x16x32_bf16 a[32:35], v[48:51], v[76:79], a[32:35]\n"
    "s_waitcnt lgkmcnt(4)\n"
    "v_accvgpr_read_b32 v89, a21\n"
    "v_accvgpr_read_b32 v90, a22\n"
    "v_mfma_f32_16x16x32_bf16 v[16:19], a[180:183], a[108:111], v[16:19]\n"
    "ds_read_b64_tr_b16 a[120:121], v235 offset:10368\n"
    "v_accvgpr_read_b32 v91, a23\n"
    "v_mfma_f32_16x16x32_bf16 v[20:23], a[184:187], a[108:111], v[20:23]\n"
    "ds_read_b64_tr_b16 a[122:123], v235 offset:11648\n"
    "v_cvt_pk_bf16_f32 v92, v84, v85\n"
    "v_mfma_f32_16x16x32_bf16 v[24:27], a[188:191], a[108:111], v[24:27]\n"
    "v_cvt_pk_bf16_f32 v93, v86, v87\n"
    "v_cvt_pk_bf16_f32 v94, v88, v89\n"
    "v_mfma_f32_16x16x32_bf16 a[24:27], v[44:47], v[64:67], a[24:27]\n"
    "v_cvt_pk_bf16_f32 v95, v90, v91\n"
    "v_mfma_f32_16x16x32_bf16 a[28:31], v[44:47], v[72:75], a[28:31]\n"
    "buffer_store_dwordx4 v[92:95], v237, s[28:31], s10 offen offset:0 sc1\n"
    "v_mfma_f32_16x16x32_bf16 a[32:35], v[44:47], v[80:83], a[32:35]\n"
    "s_waitcnt lgkmcnt(4)\n"
    "v_mfma_f32_16x16x32_bf16 v[16:19], a[192:195], a[112:115], v[16:19]\n"
    "ds_read_b64_tr_b16 a[124:125], v233 offset:20608\n"
    "s_nop 0\n"
    "v_accvgpr_read_b32 v112, a24\n"
    "v_mfma_f32_16x16x32_bf16 v[20:23], a[196:199], a[112:115], v[20:23]\n"
    "ds_read_b64_tr_b16 a[126:127], v233 offset:21888\n"
    "v_accvgpr_read_b32 v113, a25\n"
    "v_mfma_f32_16x16x32_bf16 v[24:27], a[200:203], a[112:115], v[24:27]\n"
    "s_waitcnt lgkmcnt(4)\n"
    "v_accvgpr_read_b32 v114, a26\n"
    "v_accvgpr_read_b32 v115, a27\n"
    "v_mfma_f32_16x16x32_bf16 v[4:7], a[204:207], a[116:119], 0\n"
    "ds_read_b64_tr_b16 a[128:129], v233 offset:30848\n"
    "v_accvgpr_read_b32 v116, a28\n"
    "v_mfma_f32_16x16x32_bf16 v[8:11], a[208:211], a[116:119], 0\n"
    "ds_read_b64_tr_b16 a[130:131], v233 offset:32128\n"
    "v_cvt_pk_bf16_f32 v58, v16, v17\n"
    "v_mfma_f32_16x16x32_bf16 v[12:15], a[212:215], a[116:119], 0\n"
    "s_waitcnt lgkmcnt(4)\n"
    "v_cvt_pk_bf16_f32 v59, v18, v19\n"
    "v_accvgpr_read_b32 v117, a29\n"
    "v_mfma_f32_16x16x32_bf16 v[4:7], a[216:219], a[120:123], v[4:7]\n"
    "ds_read_b64_tr_b16 a[108:109], v233 offset:41088\n"
    "v_cvt_pk_bf16_f32 v52, v20, v21\n"
    "v_mfma_f32_16x16x32_bf16 v[8:11], a[220:223], a[120:123], v[8:11]\n"
    "ds_read_b64_tr_b16 a[110:111], v233 offset:42368\n"
    "v_cvt_pk_bf16_f32 v53, v22, v23\n"
    "v_cvt_pk_bf16_f32 v54, v24, v25\n"
    "v_cvt_pk_bf16_f32 v55, v26, v27\n"
    "v_mfma_f32_16x16x32_bf16 v[12:15], a[224:227], a[120:123], v[12:15]\n"
    "v_accvgpr_read_b32 v118, a30\n"
    "v_accvgpr_read_b32 v119, a31\n"
    "v_mfma_f32_16x16x32_bf16 a[36:39], v[56:59], v[60:63], a[36:39]\n"
    "v_cvt_pk_bf16_f32 v120, v112, v113\n"
    "v_cvt_pk_bf16_f32 v121, v114, v115\n"
    "v_mfma_f32_16x16x32_bf16 a[40:43], v[56:59], v[68:71], a[40:43]\n"
    "v_cvt_pk_bf16_f32 v122, v116, v117\n"
    "v_cvt_pk_bf16_f32 v123, v118, v119\n"
    "v_mfma_f32_16x16x32_bf16 a[44:47], v[56:59], v[76:79], a[44:47]\n"
    "s_waitcnt lgkmcnt(4)\n"
    "buffer_store_dwordx4 v[120:123], v237, s[28:31], s11 offen offset:0 sc1\n"
    "v_mfma_f32_16x16x32_bf16 v[4:7], a[228:231], a[124:127], v[4:7]\n"
    "ds_read_b64_tr_b16 a[112:113], v233 offset:51328\n"
    "v_mfma_f32_16x16x32_bf16 v[8:11], a[232:235], a[124:127], v[8:11]\n"
    "ds_read_b64_tr_b16 a[114:115], v233 offset:52608\n"
    "v_mfma_f32_16x16x32_bf16 v[12:15], a[236:239], a[124:127], v[12:15]\n"
    "v_mfma_f32_16x16x32_bf16 a[36:39], v[52:55], v[64:67], a[36:39]\n"
    "v_mfma_f32_16x16x32_bf16 a[40:43], v[52:55], v[72:75], a[40:43]\n"
    "v_mfma_f32_16x16x32_bf16 a[44:47], v[52:55], v[80:83], a[44:47]\n"
    "s_waitcnt lgkmcnt(4)\n"
    "v_accvgpr_read_b32 v124, a32\n"
    "v_accvgpr_read_b32 v125, a33\n"
    "v_mfma_f32_16x16x32_bf16 v[4:7], a[240:243], a[128:131], v[4:7]\n"
    "ds_read_b64_tr_b16 a[116:117], v233 offset:128\n"
    "v_accvgpr_read_b32 v126, a34\n"
    "v_mfma_f32_16x16x32_bf16 v[8:11], a[244:247], a[128:131], v[8:11]\n"
    "ds_read_b64_tr_b16 a[118:119], v233 offset:1408\n"
    "v_accvgpr_read_b32 v127, a35\n"
    "v_mfma_f32_16x16x32_bf16 v[12:15], a[248:251], a[128:131], v[12:15]\n"
    "s_waitcnt lgkmcnt(4)\n"
    "v_accvgpr_read_b32 v128, a36\n"
    "v_accvgpr_read_b32 v129, a37\n"
    "v_mfma_f32_16x16x32_bf16 v[16:19], a[156:159], a[108:111], 0\n"
    "ds_read_b64_tr_b16 a[120:121], v233 offset:10368\n"
    "v_accvgpr_read_b32 v130, a38\n"
    "v_mfma_f32_16x16x32_bf16 v[20:23], a[160:163], a[108:111], 0\n"
    "ds_read_b64_tr_b16 a[122:123], v233 offset:11648\n"
    "v_cvt_pk_bf16_f32 v144, v4, v5\n"
    "v_mfma_f32_16x16x32_bf16 v[24:27], a[164:167], a[108:111], 0\n"
    "s_waitcnt lgkmcnt(4)\n"
    "v_cvt_pk_bf16_f32 v145, v6, v7\n"
    "v_accvgpr_read_b32 v131, a39\n"
    "v_mfma_f32_16x16x32_bf16 v[16:19], a[168:171], a[112:115], v[16:19]\n"
    "ds_read_b64_tr_b16 a[124:125], v236 offset:160\n"
    "v_cvt_pk_bf16_f32 v146, v8, v9\n"
    "v_mfma_f32_16x16x32_bf16 v[20:23], a[172:175], a[112:115], v[20:23]\n"
    "ds_read_b64_tr_b16 a[126:127], v236 offset:1440\n"
    "v_cvt_pk_bf16_f32 v147, v10, v11\n"
    "v_mfma_f32_16x16x32_bf16 v[24:27], a[176:179], a[112:115], v[24:27]\n"
    "s_waitcnt lgkmcnt(4)\n"
    "v_cvt_pk_bf16_f32 v148, v12, v13\n"
    "v_cvt_pk_bf16_f32 v132, v124, v125\n"
    "v_mfma_f32_16x16x32_bf16 v[16:19], a[180:183], a[116:119], v[16:19]\n"
    "ds_read_b64_tr_b16 a[128:129], v236 offset:10400\n"
    "v_cvt_pk_bf16_f32 v149, v14, v15\n"
    "v_mfma_f32_16x16x32_bf16 v[20:23], a[184:187], a[116:119], v[20:23]\n"
    "ds_read_b64_tr_b16 a[130:131], v236 offset:11680\n"
    "v_cvt_pk_bf16_f32 v133, v126, v127\n"
    "v_mfma_f32_16x16x32_bf16 v[24:27], a[188:191], a[116:119], v[24:27]\n"
    "v_cvt_pk_bf16_f32 v134, v128, v129\n"
    "v_cvt_pk_bf16_f32 v135, v130, v131\n"
    "v_mfma_f32_16x16x32_bf16 a[48:51], v[144:147], v[184:187], a[48:51]\n"
    "buffer_store_dwordx4 v[132:135], v237, s[28:31], s12 offen offset:0 sc1\n"
    "v_accvgpr_read_b32 v84, a40\n"
    "v_mfma_f32_16x16x32_bf16 a[52:55], v[144:147], v[196:199], a[52:55]\n"
    "v_accvgpr_read_b32 v85, a41\n"
    "v_accvgpr_read_b32 v86, a42\n"
    "v_mfma_f32_16x16x32_bf16 a[56:59], v[144:147], v[208:211], a[56:59]\n"
    "s_waitcnt lgkmcnt(4)\n"
    "v_accvgpr_read_b32 v87, a43\n"
    "v_accvgpr_read_b32 v88, a44\n"
    "v_mfma_f32_16x16x32_bf16 v[16:19], a[192:195], a[120:123], v[16:19]\n"
    "ds_read_b64_tr_b16 a[108:109], v234 offset:20640\n"
    "v_accvgpr_read_b32 v89, a45\n"
    "v_mfma_f32_16x16x32_bf16 v[20:23], a[196:199], a[120:123], v[20:23]\n"
    "ds_read_b64_tr_b16 a[110:111], v234 offset:21920\n"
    "v_accvgpr_read_b32 v90, a46\n"
    "v_mfma_f32_16x16x32_bf16 v[24:27], a[200:203], a[120:123], v[24:27]\n"
    "s_waitcnt lgkmcnt(4)\n"
    "v_accvgpr_read_b32 v91, a47\n"
    "v_cvt_pk_bf16_f32 v92, v84, v85\n"
    "v_mfma_f32_16x16x32_bf16 v[4:7], a[204:207], a[124:127], 0\n"
    "ds_read_b64_tr_b16 a[112:113], v234 offset:30880\n"
    "v_cvt_pk_bf16_f32 v93, v86, v87\n"
    "v_mfma_f32_16x16x32_bf16 v[8:11], a[208:211], a[124:127], 0\n"
    "ds_read_b64_tr_b16 a[114:115], v234 offset:32160\n"
    "v_cvt_pk_bf16_f32 v150, v16, v17\n"
    "v_mfma_f32_16x16x32_bf16 v[12:15], a[212:215], a[124:127], 0\n"
    "s_waitcnt lgkmcnt(4)\n"
    "v_cvt_pk_bf16_f32 v151, v18, v19\n"
    "v_cvt_pk_bf16_f32 v94, v88, v89\n"
    "v_mfma_f32_16x16x32_bf16 v[4:7], a[216:219], a[128:131], v[4:7]\n"
    "ds_read_b64_tr_b16 a[116:117], v234 offset:41120\n"
    "v_cvt_pk_bf16_f32 v144, v20, v21\n"
    "v_mfma_f32_16x16x32_bf16 v[8:11], a[220:223], a[128:131], v[8:11]\n"
    "ds_read_b64_tr_b16 a[118:119], v234 offset:42400\n"
    "v_cvt_pk_bf16_f32 v145, v22, v23\n"
    "v_cvt_pk_bf16_f32 v146, v24, v25\n"
    "v_cvt_pk_bf16_f32 v147, v26, v27\n"
    "v_mfma_f32_16x16x32_bf16 v[12:15], a[224:227], a[128:131], v[12:15]\n"
    "v_cvt_pk_bf16_f32 v95, v90, v91\n"
    "buffer_store_dwordx4 v[92:95], v237, s[28:31], s13 offen offset:0 sc1\n"
    "v_mfma_f32_16x16x32_bf16 a[48:51], v[148:151], v[60:63], a[48:51]\n"
    "v_mfma_f32_16x16x32_bf16 a[52:55], v[148:151], v[68:71], a[52:55]\n"
    "v_mfma_f32_16x16x32_bf16 a[56:59], v[148:151], v[76:79], a[56:59]\n"
    "s_waitcnt lgkmcnt(4)\n"
    "v_mfma_f32_16x16x32_bf16 v[4:7], a[228:231], a[108:111], v[4:7]\n"
    "ds_read_b64_tr_b16 a[120:121], v234 offset:51360\n"
    "v_mfma_f32_16x16x32_bf16 v[8:11], a[232:235], a[108:111], v[8:11]\n"
    "ds_read_b64_tr_b16 a[122:123], v234 offset:52640\n"
    "v_mfma_f32_16x16x32_bf16 v[12:15], a[236:239], a[108:111], v[12:15]\n"
    "v_mfma_f32_16x16x32_bf16 a[48:51], v[144:147], v[64:67], a[48:51]\n"
    "v_mfma_f32_16x16x32_bf16 a[52:55], v[144:147], v[72:75], a[52:55]\n"
    "v_mfma_f32_16x16x32_bf16 a[56:59], v[144:147], v[80:83], a[56:59]\n"
    "s_waitcnt lgkmcnt(4)\n"
    "v_mfma_f32_16x16x32_bf16 v[4:7], a[240:243], a[112:115], v[4:7]\n"
    "ds_read_b64_tr_b16 a[124:125], v234 offset:160\n"
    "s_nop 2\n"
    "v_accvgpr_read_b32 v112, a48\n"
    "v_mfma_f32_16x16x32_bf16 v[8:11], a[244:247], a[112:115], v[8:11]\n"
    "ds_read_b64_tr_b16 a[126:127], v234 offset:1440\n"
    "v_accvgpr_read_b32 v113, a49\n"
    "v_mfma_f32_16x16x32_bf16 v[12:15], a[248:251], a[112:115], v[12:15]\n"
    "s_waitcnt lgkmcnt(4)\n"
    "v_accvgpr_read_b32 v114, a50\n"
    "v_accvgpr_read_b32 v115, a51\n"
    "v_mfma_f32_16x16x32_bf16 v[16:19], a[156:159], a[116:119], 0\n"
    "ds_read_b64_tr_b16 a[128:129], v234 offset:10400\n"
    "v_accvgpr_read_b32 v116, a52\n"
    "v_mfma_f32_16x16x32_bf16 v[20:23], a[160:163], a[116:119], 0\n"
    "ds_read_b64_tr_b16 a[130:131], v234 offset:11680\n"
    "v_cvt_pk_bf16_f32 v152, v4, v5\n"
    "v_mfma_f32_16x16x32_bf16 v[24:27], a[164:167], a[116:119], 0\n"
    "s_waitcnt lgkmcnt(4)\n"
    "v_cvt_pk_bf16_f32 v153, v6, v7\n"
    "v_accvgpr_read_b32 v117, a53\n"
    "v_mfma_f32_16x16x32_bf16 v[16:19], a[168:171], a[120:123], v[16:19]\n"
    "ds_read_b64_tr_b16 a[108:109], v235 offset:192\n"
    "v_cvt_pk_bf16_f32 v154, v8, v9\n"
    "v_mfma_f32_16x16x32_bf16 v[20:23], a[172:175], a[120:123], v[20:23]\n"
    "ds_read_b64_tr_b16 a[110:111], v235 offset:1472\n"
    "v_cvt_pk_bf16_f32 v155, v10, v11\n"
    "v_mfma_f32_16x16x32_bf16 v[24:27], a[176:179], a[120:123], v[24:27]\n"
    "s_waitcnt lgkmcnt(4)\n"
    "v_cvt_pk_bf16_f32 v156, v12, v13\n"
    "v_accvgpr_read_b32 v118, a54\n"
    "v_mfma_f32_16x16x32_bf16 v[16:19], a[180:183], a[124:127], v[16:19]\n"
    "ds_read_b64_tr_b16 a[112:113], v235 offset:10432\n"
    "v_cvt_pk_bf16_f32 v157, v14, v15\n"
    "v_mfma_f32_16x16x32_bf16 v[20:23], a[184:187], a[124:127], v[20:23]\n"
    "ds_read_b64_tr_b16 a[114:115], v235 offset:11712\n"
    "v_accvgpr_read_b32 v119, a55\n"
    "v_mfma_f32_16x16x32_bf16 v[24:27], a[188:191], a[124:127], v[24:27]\n"
    "v_cvt_pk_bf16_f32 v120, v112, v113\n"
    "v_cvt_pk_bf16_f32 v121, v114, v115\n"
    "v_mfma_f32_16x16x32_bf16 a[60:63], v[152:155], v[184:187], a[60:63]\n"
    "v_cvt_pk_bf16_f32 v122, v116, v117\n"
    "v_cvt_pk_bf16_f32 v123, v118, v119\n"
    "v_mfma_f32_16x16x32_bf16 a[64:67], v[152:155], v[196:199], a[64:67]\n"
    "buffer_store_dwordx4 v[120:123], v237, s[28:31], s14 offen offset:0 sc1\n"
    "v_mfma_f32_16x16x32_bf16 a[68:71], v[152:155], v[208:211], a[68:71]\n"
    "s_waitcnt lgkmcnt(4)\n"
    "v_mfma_f32_16x16x32_bf16 v[16:19], a[192:195], a[128:131], v[16:19]\n"
    "ds_read_b64_tr_b16 a[116:117], v233 offset:20672\n"
    "v_mfma_f32_16x16x32_bf16 v[20:23], a[196:199], a[128:131], v[20:23]\n"
    "ds_read_b64_tr_b16 a[118:119], v233 offset:21952\n"
    "v_mfma_f32_16x16x32_bf16 v[24:27], a[200:203], a[128:131], v[24:27]\n"
    "s_waitcnt lgkmcnt(4)\n"
    "v_mfma_f32_16x16x32_bf16 v[4:7], a[204:207], a[108:111], 0\n"
    "ds_read_b64_tr_b16 a[120:121], v233 offset:30912\n"
    "v_mfma_f32_16x16x32_bf16 v[8:11], a[208:211], a[108:111], 0\n"
    "ds_read_b64_tr_b16 a[122:123], v233 offset:32192\n"
    "v_cvt_pk_bf16_f32 v158, v16, v17\n"
    "v_mfma_f32_16x16x32_bf16 v[12:15], a[212:215], a[108:111], 0\n"
    "s_waitcnt lgkmcnt(4)\n"
    "v_cvt_pk_bf16_f32 v159, v18, v19\n"
    "v_cvt_pk_bf16_f32 v152, v20, v21\n"
    "v_mfma_f32_16x16x32_bf16 v[4:7], a[216:219], a[112:115], v[4:7]\n"
    "s_waitcnt vmcnt(1)\n"
    "ds_read_b64_tr_b16 a[124:125], v233 offset:41152\n"
    "v_cvt_pk_bf16_f32 v153, v22, v23\n"
    "v_mfma_f32_16x16x32_bf16 v[8:11], a[220:223], a[112:115], v[8:11]\n"
    "s_add_u32 s81, s59, 135168\n"
    "ds_read_b64_tr_b16 a[126:127], v233 offset:42432\n"
    "v_cvt_pk_bf16_f32 v154, v24, v25\n"
    "v_cvt_pk_bf16_f32 v155, v26, v27\n"
    "v_mfma_f32_16x16x32_bf16 v[12:15], a[224:227], a[112:115], v[12:15]\n"
    "buffer_store_dword v249, v240, s[28:31], s81 offen offset:0 sc1\n"
    "v_mfma_f32_16x16x32_bf16 a[60:63], v[156:159], v[60:63], a[60:63]\n"
    "v_mfma_f32_16x16x32_bf16 a[64:67], v[156:159], v[68:71], a[64:67]\n"
    "v_mfma_f32_16x16x32_bf16 a[68:71], v[156:159], v[76:79], a[68:71]\n"
    "s_waitcnt lgkmcnt(4)\n"
    "v_mfma_f32_16x16x32_bf16 v[4:7], a[228:231], a[116:119], v[4:7]\n"
    "ds_read_b64_tr_b16 a[128:129], v233 offset:51392\n"
    "v_mfma_f32_16x16x32_bf16 v[8:11], a[232:235], a[116:119], v[8:11]\n"
    "ds_read_b64_tr_b16 a[130:131], v233 offset:52672\n"
    "v_mfma_f32_16x16x32_bf16 v[12:15], a[236:239], a[116:119], v[12:15]\n"
    "v_mfma_f32_16x16x32_bf16 a[60:63], v[152:155], v[64:67], a[60:63]\n"
    "v_mfma_f32_16x16x32_bf16 a[64:67], v[152:155], v[72:75], a[64:67]\n"
    "v_mfma_f32_16x16x32_bf16 a[68:71], v[152:155], v[80:83], a[68:71]\n"
    "s_waitcnt lgkmcnt(4)\n"
    "v_accvgpr_read_b32 v124, a56\n"
    "v_accvgpr_read_b32 v125, a57\n"
    "v_mfma_f32_16x16x32_bf16 v[4:7], a[240:243], a[120:123], v[4:7]\n"
    "ds_read_b64_tr_b16 a[108:109], v233 offset:192\n"
    "v_accvgpr_read_b32 v126, a58\n"
    "v_mfma_f32_16x16x32_bf16 v[8:11], a[244:247], a[120:123], v[8:11]\n"
    "ds_read_b64_tr_b16 a[110:111], v233 offset:1472\n"
    "v_accvgpr_read_b32 v127, a59\n"
    "v_mfma_f32_16x16x32_bf16 v[12:15], a[248:251], a[120:123], v[12:15]\n"
    "s_waitcnt lgkmcnt(4)\n"
    "v_accvgpr_read_b32 v128, a60\n"
    "v_accvgpr_read_b32 v129, a61\n"
    "v_mfma_f32_16x16x32_bf16 v[16:19], a[156:159], a[124:127], 0\n"
    "ds_read_b64_tr_b16 a[112:113], v233 offset:10432\n"
    "v_accvgpr_read_b32 v130, a62\n"
    "v_mfma_f32_16x16x32_bf16 v[20:23], a[160:163], a[124:127], 0\n"
    "ds_read_b64_tr_b16 a[114:115], v233 offset:11712\n"
    "v_cvt_pk_bf16_f32 v160, v4, v5\n"
    "v_mfma_f32_16x16x32_bf16 v[24:27], a[164:167], a[124:127], 0\n"
    "s_waitcnt lgkmcnt(4)\n"
    "v_cvt_pk_bf16_f32 v161, v6, v7\n"
    "v_accvgpr_read_b32 v131, a63\n"
    "v_mfma_f32_16x16x32_bf16 v[16:19], a[168:171], a[128:131], v[16:19]\n"
    "ds_read_b64_tr_b16 a[116:117], v236 offset:224\n"
    "v_cvt_pk_bf16_f32 v162, v8, v9\n"
    "v_mfma_f32_16x16x32_bf16 v[20:23], a[172:175], a[128:131], v[20:23]\n"
    "ds_read_b64_tr_b16 a[118:119], v236 offset:1504\n"
    "v_cvt_pk_bf16_f32 v163, v10, v11\n"
    "v_mfma_f32_16x16x32_bf16 v[24:27], a[176:179], a[128:131], v[24:27]\n"
    "s_waitcnt lgkmcnt(4)\n"
    "v_cvt_pk_bf16_f32 v164, v12, v13\n"
    "v_cvt_pk_bf16_f32 v132, v124, v125\n"
    "v_mfma_f32_16x16x32_bf16 v[16:19], a[180:183], a[108:111], v[16:19]\n"
    "ds_read_b64_tr_b16 a[120:121], v236 offset:10464\n"
    "v_cvt_pk_bf16_f32 v165, v14, v15\n"
    "v_mfma_f32_16x16x32_bf16 v[20:23], a[184:187], a[108:111], v[20:23]\n"
    "ds_read_b64_tr_b16 a[122:123], v236 offset:11744\n"
    "v_cvt_pk_bf16_f32 v133, v126, v127\n"
    "v_mfma_f32_16x16x32_bf16 v[24:27], a[188:191], a[108:111], v[24:27]\n"
    "v_cvt_pk_bf16_f32 v134, v128, v129\n"
    "v_cvt_pk_bf16_f32 v135, v130, v131\n"
    "v_mfma_f32_16x16x32_bf16 a[72:75], v[160:163], v[184:187], a[72:75]\n"
    "buffer_store_dwordx4 v[132:135], v237, s[28:31], s15 offen offset:0 sc1\n"
    "v_accvgpr_read_b32 v84, a64\n"
    "v_mfma_f32_16x16x32_bf16 a[76:79], v[160:163], v[196:199], a[76:79]\n"
    "v_accvgpr_read_b32 v85, a65\n"
    "v_accvgpr_read_b32 v86, a66\n"
    "v_mfma_f32_16x16x32_bf16 a[80:83], v[160:163], v[208:211], a[80:83]\n"
    "s_waitcnt lgkmcnt(4)\n"
    "v_accvgpr_read_b32 v87, a67\n"
    "v_accvgpr_read_b32 v88, a68\n"
    "v_mfma_f32_16x16x32_bf16 v[16:19], a[192:195], a[112:115], v[16:19]\n"
    "ds_read_b64_tr_b16 a[124:125], v234 offset:20704\n"
    "v_accvgpr_read_b32 v89, a69\n"
    "v_mfma_f32_16x16x32_bf16 v[20:23], a[196:199], a[112:115], v[20:23]\n"
    "ds_read_b64_tr_b16 a[126:127], v234 offset:21984\n"
    "v_accvgpr_read_b32 v90, a70\n"
    "v_mfma_f32_16x16x32_bf16 v[24:27], a[200:203], a[112:115], v[24:27]\n"
    "s_waitcnt lgkmcnt(4)\n"
    "v_accvgpr_read_b32 v91, a71\n"
    "v_cvt_pk_bf16_f32 v92, v84, v85\n"
    "v_mfma_f32_16x16x32_bf16 v[4:7], a[204:207], a[116:119], 0\n"
    "ds_read_b64_tr_b16 a[128:129], v234 offset:30944\n"
    "v_cvt_pk_bf16_f32 v93, v86, v87\n"
    "v_mfma_f32_16x16x32_bf16 v[8:11], a[208:211], a[116:119], 0\n"
    "ds_read_b64_tr_b16 a[130:131], v234 offset:32224\n"
    "v_cvt_pk_bf16_f32 v166, v16, v17\n"
    "v_mfma_f32_16x16x32_bf16 v[12:15], a[212:215], a[116:119], 0\n"
    "s_waitcnt lgkmcnt(4)\n"
    "v_cvt_pk_bf16_f32 v167, v18, v19\n"
    "v_cvt_pk_bf16_f32 v94, v88, v89\n"
    "v_mfma_f32_16x16x32_bf16 v[4:7], a[216:219], a[120:123], v[4:7]\n"
    "ds_read_b64_tr_b16 a[108:109], v234 offset:41184\n"
    "v_cvt_pk_bf16_f32 v160, v20, v21\n"
    "v_mfma_f32_16x16x32_bf16 v[8:11], a[220:223], a[120:123], v[8:11]\n"
    "ds_read_b64_tr_b16 a[110:111], v234 offset:42464\n"
    "v_cvt_pk_bf16_f32 v161, v22, v23\n"
    "v_cvt_pk_bf16_f32 v162, v24, v25\n"
    "v_cvt_pk_bf16_f32 v163, v26, v27\n"
    "v_mfma_f32_16x16x32_bf16 v[12:15], a[224:227], a[120:123], v[12:15]\n"
    "v_cvt_pk_bf16_f32 v95, v90, v91\n"
    "v_mfma_f32_16x16x32_bf16 a[72:75], v[164:167], v[60:63], a[72:75]\n"
    "buffer_store_dwordx4 v[92:95], v237, s[28:31], s20 offen offset:0 sc1\n"
    "v_mfma_f32_16x16x32_bf16 a[76:79], v[164:167], v[68:71], a[76:79]\n"
    "v_mfma_f32_16x16x32_bf16 a[80:83], v[164:167], v[76:79], a[80:83]\n"
    "s_waitcnt lgkmcnt(4)\n"
    "v_mfma_f32_16x16x32_bf16 v[4:7], a[228:231], a[124:127], v[4:7]\n"
    "ds_read_b64_tr_b16 a[112:113], v234 offset:51424\n"
    "v_mfma_f32_16x16x32_bf16 v[8:11], a[232:235], a[124:127], v[8:11]\n"
    "ds_read_b64_tr_b16 a[114:115], v234 offset:52704\n"
    "v_mfma_f32_16x16x32_bf16 v[12:15], a[236:239], a[124:127], v[12:15]\n"
    "v_mfma_f32_16x16x32_bf16 a[72:75], v[160:163], v[64:67], a[72:75]\n"
    "v_mfma_f32_16x16x32_bf16 a[76:79], v[160:163], v[72:75], a[76:79]\n"
    "v_mfma_f32_16x16x32_bf16 a[80:83], v[160:163], v[80:83], a[80:83]\n"
    "s_waitcnt lgkmcnt(4)\n"
    "v_mfma_f32_16x16x32_bf16 v[4:7], a[240:243], a[128:131], v[4:7]\n"
    "ds_read_b64_tr_b16 a[116:117], v234 offset:224\n"
    "s_nop 2\n"
    "v_accvgpr_read_b32 v112, a72\n"
    "v_mfma_f32_16x16x32_bf16 v[8:11], a[244:247], a[128:131], v[8:11]\n"
    "ds_read_b64_tr_b16 a[118:119], v234 offset:1504\n"
    "v_accvgpr_read_b32 v113, a73\n"
    "v_mfma_f32_16x16x32_bf16 v[12:15], a[248:251], a[128:131], v[12:15]\n"
    "s_waitcnt lgkmcnt(4)\n"
    "v_accvgpr_read_b32 v114, a74\n"
    "v_accvgpr_read_b32 v115, a75\n"
    "v_mfma_f32_16x16x32_bf16 v[16:19], a[156:159], a[108:111], 0\n"
    "ds_read_b64_tr_b16 a[120:121], v234 offset:10464\n"
    "v_accvgpr_read_b32 v116, a76\n"
    "v_mfma_f32_16x16x32_bf16 v[20:23], a[160:163], a[108:111], 0\n"
    "ds_read_b64_tr_b16 a[122:123], v234 offset:11744\n"
    "v_cvt_pk_bf16_f32 v168, v4, v5\n"
    "v_mfma_f32_16x16x32_bf16 v[24:27], a[164:167], a[108:111], 0\n"
    "s_waitcnt lgkmcnt(4)\n"
    "v_cvt_pk_bf16_f32 v169, v6, v7\n"
    "v_accvgpr_read_b32 v117, a77\n"
    "v_mfma_f32_16x16x32_bf16 v[16:19], a[168:171], a[112:115], v[16:19]\n"
    "ds_read_b64_tr_b16 a[124:125], v235 offset:256\n"
    "v_cvt_pk_bf16_f32 v170, v8, v9\n"
    "v_mfma_f32_16x16x32_bf16 v[20:23], a[172:175], a[112:115], v[20:23]\n"
    "ds_read_b64_tr_b16 a[126:127], v235 offset:1536\n"
    "v_cvt_pk_bf16_f32 v171, v10, v11\n"
    "v_mfma_f32_16x16x32_bf16 v[24:27], a[176:179], a[112:115], v[24:27]\n"
    "s_waitcnt lgkmcnt(4)\n"
    "v_cvt_pk_bf16_f32 v172, v12, v13\n"
    "v_accvgpr_read_b32 v118, a78\n"
    "v_mfma_f32_16x16x32_bf16 v[16:19], a[180:183], a[116:119], v[16:19]\n"
    "ds_read_b64_tr_b16 a[128:129], v235 offset:10496\n"
    "v_cvt_pk_bf16_f32 v173, v14, v15\n"
    "v_mfma_f32_16x16x32_bf16 v[20:23], a[184:187], a[116:119], v[20:23]\n"
    "ds_read_b64_tr_b16 a[130:131], v235 offset:11776\n"
    "v_accvgpr_read_b32 v119, a79\n"
    "v_mfma_f32_16x16x32_bf16 v[24:27], a[188:191], a[116:119], v[24:27]\n"
    "v_cvt_pk_bf16_f32 v120, v112, v113\n"
    "v_cvt_pk_bf16_f32 v121, v114, v115\n"
    "v_mfma_f32_16x16x32_bf16 a[84:87], v[168:171], v[184:187], a[84:87]\n"
    "v_cvt_pk_bf16_f32 v122, v116, v117\n"
    "v_cvt_pk_bf16_f32 v123, v118, v119\n"
    "v_mfma_f32_16x16x32_bf16 a[88:91], v[168:171], v[196:199], a[88:91]\n"
    "buffer_store_dwordx4 v[120:123], v237, s[28:31], s21 offen offset:0 sc1\n"
    "v_mfma_f32_16x16x32_bf16 a[92:95], v[168:171], v[208:211], a[92:95]\n"
    "s_waitcnt lgkmcnt(4)\n"
    "v_mfma_f32_16x16x32_bf16 v[16:19], a[192:195], a[120:123], v[16:19]\n"
    "ds_read_b64_tr_b16 a[108:109], v233 offset:20736\n"
    "v_mfma_f32_16x16x32_bf16 v[20:23], a[196:199], a[120:123], v[20:23]\n"
    "ds_read_b64_tr_b16 a[110:111], v233 offset:22016\n"
    "v_mfma_f32_16x16x32_bf16 v[24:27], a[200:203], a[120:123], v[24:27]\n"
    "s_waitcnt lgkmcnt(4)\n"
    "v_mfma_f32_16x16x32_bf16 v[4:7], a[204:207], a[124:127], 0\n"
    "ds_read_b64_tr_b16 a[112:113], v233 offset:30976\n"
    "v_mfma_f32_16x16x32_bf16 v[8:11], a[208:211], a[124:127], 0\n"
    "ds_read_b64_tr_b16 a[114:115], v233 offset:32256\n"
    "v_cvt_pk_bf16_f32 v174, v16, v17\n"
    "v_mfma_f32_16x16x32_bf16 v[12:15], a[212:215], a[124:127], 0\n"
    "s_waitcnt lgkmcnt(4)\n"
    "v_cvt_pk_bf16_f32 v175, v18, v19\n"
    "v_cvt_pk_bf16_f32 v168, v20, v21\n"
    "v_mfma_f32_16x16x32_bf16 v[4:7], a[216:219], a[128:131], v[4:7]\n"
    "ds_read_b64_tr_b16 a[116:117], v233 offset:41216\n"
    "v_cvt_pk_bf16_f32 v169, v22, v23\n"
    "v_mfma_f32_16x16x32_bf16 v[8:11], a[220:223], a[128:131], v[8:11]\n"
    "ds_read_b64_tr_b16 a[118:119], v233 offset:42496\n"
    "v_cvt_pk_bf16_f32 v170, v24, v25\n"
    "v_cvt_pk_bf16_f32 v171, v26, v27\n"
    "v_mfma_f32_16x16x32_bf16 v[12:15], a[224:227], a[128:131], v[12:15]\n"
    "v_mfma_f32_16x16x32_bf16 a[84:87], v[172:175], v[60:63], a[84:87]\n"
    "v_mfma_f32_16x16x32_bf16 a[88:91], v[172:175], v[68:71], a[88:91]\n"
    "v_mfma_f32_16x16x32_bf16 a[92:95], v[172:175], v[76:79], a[92:95]\n"
    "s_waitcnt lgkmcnt(4)\n"
    "v_mfma_f32_16x16x32_bf16 v[4:7], a[228:231], a[108:111], v[4:7]\n"
    "ds_read_b64_tr_b16 a[120:121], v233 offset:51456\n"
    "buffer_load_dword v248, v242, s[28:31], 0 offen offset:0 sc1\n"
    "v_mfma_f32_16x16x32_bf16 v[8:11], a[232:235], a[108:111], v[8:11]\n"
    "ds_read_b64_tr_b16 a[122:123], v233 offset:52736\n"
    "v_mfma_f32_16x16x32_bf16 v[12:15], a[236:239], a[108:111], v[12:15]\n"
    "v_mfma_f32_16x16x32_bf16 a[84:87], v[168:171], v[64:67], a[84:87]\n"
    "v_mfma_f32_16x16x32_bf16 a[88:91], v[168:171], v[72:75], a[88:91]\n"
    "v_mfma_f32_16x16x32_bf16 a[92:95], v[168:171], v[80:83], a[92:95]\n"
    "s_waitcnt lgkmcnt(4)\n"
    "v_accvgpr_read_b32 v124, a80\n"
    "v_accvgpr_read_b32 v125, a81\n"
    "v_mfma_f32_16x16x32_bf16 v[4:7], a[240:243], a[112:115], v[4:7]\n"
    "ds_read_b64_tr_b16 a[124:125], v233 offset:256\n"
    "v_accvgpr_read_b32 v126, a82\n"
    "v_mfma_f32_16x16x32_bf16 v[8:11], a[244:247], a[112:115], v[8:11]\n"
    "ds_read_b64_tr_b16 a[126:127], v233 offset:1536\n"
    "v_accvgpr_read_b32 v127, a83\n"
    "v_mfma_f32_16x16x32_bf16 v[12:15], a[248:251], a[112:115], v[12:15]\n"
    "s_waitcnt lgkmcnt(4)\n"
    "v_accvgpr_read_b32 v128, a84\n"
    "v_accvgpr_read_b32 v129, a85\n"
    "v_mfma_f32_16x16x32_bf16 v[16:19], a[156:159], a[116:119], 0\n"
    "ds_read_b64_tr_b16 a[128:129], v233 offset:10496\n"
    "v_accvgpr_read_b32 v130, a86\n"
    "v_mfma_f32_16x16x32_bf16 v[20:23], a[160:163], a[116:119], 0\n"
    "ds_read_b64_tr_b16 a[130:131], v233 offset:11776\n"
    "v_cvt_pk_bf16_f32 v176, v4, v5\n"
    "v_mfma_f32_16x16x32_bf16 v[24:27], a[164:167], a[116:119], 0\n"
    "s_waitcnt lgkmcnt(4)\n"
    "v_cvt_pk_bf16_f32 v177, v6, v7\n"
    "v_accvgpr_read_b32 v131, a87\n"
    "v_mfma_f32_16x16x32_bf16 v[16:19], a[168:171], a[120:123], v[16:19]\n"
    "v_cvt_pk_bf16_f32 v178, v8, v9\n"
    "v_cvt_pk_bf16_f32 v132, v124, v125\n"
    "v_mfma_f32_16x16x32_bf16 v[20:23], a[172:175], a[120:123], v[20:23]\n"
    "v_cvt_pk_bf16_f32 v179, v10, v11\n"
    "v_cvt_pk_bf16_f32 v133, v126, v127\n"
    "v_mfma_f32_16x16x32_bf16 v[24:27], a[176:179], a[120:123], v[24:27]\n"
    "s_waitcnt lgkmcnt(2)\n"
    "v_cvt_pk_bf16_f32 v180, v12, v13\n"
    "v_cvt_pk_bf16_f32 v134, v128, v129\n"
    "v_mfma_f32_16x16x32_bf16 v[16:19], a[180:183], a[124:127], v[16:19]\n"
    "s_waitcnt vmcnt(1)\n"
    "v_cvt_pk_bf16_f32 v181, v14, v15\n"
    "v_cvt_pk_bf16_f32 v135, v130, v131\n"
    "v_mfma_f32_16x16x32_bf16 v[20:23], a[184:187], a[124:127], v[20:23]\n"
    "s_add_u32 s81, s59, 266240\n"
    "buffer_store_dwordx4 v[132:135], v237, s[28:31], s22 offen offset:0 sc1\n"
    "buffer_store_dword v249, v240, s[28:31], s81 offen offset:0 sc1\n"
    "v_mfma_f32_16x16x32_bf16 v[24:27], a[188:191], a[124:127], v[24:27]\n"
    "v_accvgpr_read_b32 v84, a88\n"
    "v_accvgpr_read_b32 v85, a89\n"
    "v_mfma_f32_16x16x32_bf16 a[96:99], v[176:179], v[184:187], a[96:99]\n"
    "v_accvgpr_read_b32 v86, a90\n"
    "v_accvgpr_read_b32 v87, a91\n"
    "v_mfma_f32_16x16x32_bf16 a[100:103], v[176:179], v[196:199], a[100:103]\n"
    "v_accvgpr_read_b32 v88, a92\n"
    "v_accvgpr_read_b32 v89, a93\n"
    "v_mfma_f32_16x16x32_bf16 a[104:107], v[176:179], v[208:211], a[104:107]\n"
    "s_waitcnt lgkmcnt(0)\n"
    "v_accvgpr_read_b32 v90, a94\n"
    "v_accvgpr_read_b32 v91, a95\n"
    "v_mfma_f32_16x16x32_bf16 v[16:19], a[192:195], a[128:131], v[16:19]\n"
    "v_cvt_pk_bf16_f32 v92, v84, v85\n"
    "v_cvt_pk_bf16_f32 v93, v86, v87\n"
    "v_mfma_f32_16x16x32_bf16 v[20:23], a[196:199], a[128:131], v[20:23]\n"
    "v_cvt_pk_bf16_f32 v94, v88, v89\n"
    "v_cvt_pk_bf16_f32 v95, v90, v91\n"
    "v_mfma_f32_16x16x32_bf16 v[24:27], a[200:203], a[128:131], v[24:27]\n"
    "s_nop 1\n"
    "v_cvt_pk_bf16_f32 v182, v16, v17\n"
    "v_cvt_pk_bf16_f32 v183, v18, v19\n"
    "s_nop 0\n"
    "v_cvt_pk_bf16_f32 v176, v20, v21\n"
    "v_cvt_pk_bf16_f32 v177, v22, v23\n"
    "s_nop 0\n"
    "v_cvt_pk_bf16_f32 v178, v24, v25\n"
    "v_cvt_pk_bf16_f32 v179, v26, v27\n"
    "v_mfma_f32_16x16x32_bf16 a[96:99], v[180:183], v[60:63], a[96:99]\n"
    "buffer_store_dwordx4 v[92:95], v237, s[28:31], s23 offen offset:0 sc1\n"
    "v_mfma_f32_16x16x32_bf16 a[100:103], v[180:183], v[68:71], a[100:103]\n"
    "v_mfma_f32_16x16x32_bf16 a[104:107], v[180:183], v[76:79], a[104:107]\n"
    "v_mfma_f32_16x16x32_bf16 a[96:99], v[176:179], v[64:67], a[96:99]\n"
    "v_mfma_f32_16x16x32_bf16 a[100:103], v[176:179], v[72:75], a[100:103]\n"
    "v_mfma_f32_16x16x32_bf16 a[104:107], v[176:179], v[80:83], a[104:107]\n"
    "s_nop 5\n"
    "v_accvgpr_read_b32 v112, a96\n"
    "v_accvgpr_read_b32 v113, a97\n"
    "v_accvgpr_read_b32 v114, a98\n"
    "v_accvgpr_read_b32 v115, a99\n"
    "v_accvgpr_read_b32 v116, a100\n"
    "v_accvgpr_read_b32 v117, a101\n"
    "v_accvgpr_read_b32 v118, a102\n"
    "v_accvgpr_read_b32 v119, a103\n"
    "v_cvt_pk_bf16_f32 v120, v112, v113\n"
    "v_cvt_pk_bf16_f32 v121, v114, v115\n"
    "v_cvt_pk_bf16_f32 v122, v116, v117\n"
    "v_cvt_pk_bf16_f32 v123, v118, v119\n"
    "buffer_store_dwordx4 v[120:123], v237, s[28:31], s36 offen offset:0 sc1\n"
    "v_accvgpr_read_b32 v124, a104\n"
    "v_accvgpr_read_b32 v125, a105\n"
    "v_accvgpr_read_b32 v126, a106\n"
    "v_accvgpr_read_b32 v127, a107\n"
    "v_cvt_pk_bf16_f32 v132, v124, v125\n"
    "v_cvt_pk_bf16_f32 v133, v126, v127\n"
    "v_mov_b32 v134, 0\n"
    "v_mov_b32 v135, 0\n"
    "buffer_store_dwordx4 v[132:135], v237, s[28:31], s37 offen offset:0 sc1\n"
    "s_waitcnt vmcnt(5)\n"
    "v_cmp_ne_u32 vcc, v249, v248\n"
    "s_nop 3\n"
    "s_cbranch_vccnz L_lateA_%=\n"
    "buffer_load_dwordx4 v[188:191], v237, s[28:31], s78 offen offset:0 sc1\n"
    "buffer_load_dwordx4 v[192:195], v237, s[28:31], s78 offen offset:1024 sc1\n"
    "buffer_load_dwordx4 v[200:203], v237, s[28:31], s78 offen offset:2048 sc1\n"
    "buffer_load_dwordx4 v[204:207], v237, s[28:31], s78 offen offset:3072 sc1\n"
    "buffer_load_dwordx4 v[212:215], v239, s[28:31], s78 offen offset:0 sc1\n"
    "buffer_load_dwordx4 v[216:219], v239, s[28:31], s78 offen offset:1024 sc1\n"
    "buffer_load_dwordx4 v[96:99], v239, s[28:31], s78 offen offset:2048 sc1\n"
    "s_waitcnt vmcnt(7)\n"
    "s_add_u32 s81, s59, 397312\n"
    "buffer_store_dword v249, v240, s[28:31], s81 offen offset:0 sc1\n"
    "L_joinA_%=:\n"
    "s_cmp_eq_u32 s57, 0\n"
    "s_cbranch_scc1 L_own0_%=\n"
    "s_cmp_eq_u32 s57, 1\n"
    "s_cbranch_scc1 L_own1_%=\n"
    "s_cmp_eq_u32 s57, 2\n"
    "s_cbranch_scc1 L_own2_%=\n"
    "s_cmp_eq_u32 s57, 3\n"
    "s_cbranch_scc1 L_own3_%=\n"
    "s_cmp_eq_u32 s57, 4\n"
    "s_cbranch_scc1 L_own4_%=\n"
    "s_cmp_eq_u32 s57, 5\n"
    "s_cbranch_scc1 L_own5_%=\n"
    "s_cmp_eq_u32 s57, 6\n"
    "s_cbranch_scc1 L_own6_%=\n"
    "s_branch L_own7_%=\n"
    "L_ownret_%=:\n"
    "s_waitcnt vmcnt(0) lgkmcnt(0)\n"
    "v_dot2_f32_bf16 v48, v188, s42, v4\n"
    "v_dot2_f32_bf16 v49, v188, s43, v5\n"
    "v_dot2_f32_bf16 v50, v189, s42, v6\n"
    "v_dot2_f32_bf16 v51, v189, s43, v7\n"
    "v_dot2_f32_bf16 v52, v190, s42, v8\n"
    "v_dot2_f32_bf16 v53, v190, s43, v9\n"
    "v_dot2_f32_bf16 v54, v191, s42, v10\n"
    "v_dot2_f32_bf16 v55, v191, s43, v11\n"
    "v_dot2_f32_bf16 v48, v192, s42, v48\n"
    "v_dot2_f32_bf16 v49, v192, s43, v49\n"
    "v_dot2_f32_bf16 v50, v193, s42, v50\n"
    "v_dot2_f32_bf16 v51, v193, s43, v51\n"
    "v_dot2_f32_bf16 v52, v194, s42, v52\n"
    "v_dot2_f32_bf16 v53, v194, s43, v53\n"
    "v_dot2_f32_bf16 v54, v195, s42, v54\n"
    "v_dot2_f32_bf16 v55, v195, s43, v55\n"
    "v_dot2_f32_bf16 v48, v200, s42, v48\n"
    "v_dot2_f32_bf16 v49, v200, s43, v49\n"
    "v_dot2_f32_bf16 v50, v201, s42, v50\n"
    "v_dot2_f32_bf16 v51, v201, s43, v51\n"
    "v_dot2_f32_bf16 v52, v202, s42, v52\n"
    "v_dot2_f32_bf16 v53, v202, s43, v53\n"
    "v_dot2_f32_bf16 v54, v203, s42, v54\n"
    "v_dot2_f32_bf16 v55, v203, s43, v55\n"
    "v_dot2_f32_bf16 v48, v204, s42, v48\n"
    "v_dot2_f32_bf16 v49, v204, s43, v49\n"
    "v_dot2_f32_bf16 v50, v205, s42, v50\n"
    "v_dot2_f32_bf16 v51, v205, s43, v51\n"
    "v_dot2_f32_bf16 v52, v206, s42, v52\n"
    "v_dot2_f32_bf16 v53, v206, s43, v53\n"
    "v_dot2_f32_bf16 v54, v207, s42, v54\n"
    "v_dot2_f32_bf16 v55, v207, s43, v55\n"
    "v_dot2_f32_bf16 v48, v212, s42, v48\n"
    "v_dot2_f32_bf16 v49, v212, s43, v49\n"
    "v_dot2_f32_bf16 v50, v213, s42, v50\n"
    "v_dot2_f32_bf16 v51, v213, s43, v51\n"
    "v_dot2_f32_bf16 v52, v214, s42, v52\n"
    "v_dot2_f32_bf16 v53, v214, s43, v53\n"
    "v_dot2_f32_bf16 v54, v215, s42, v54\n"
    "v_dot2_f32_bf16 v55, v215, s43, v55\n"
    "v_dot2_f32_bf16 v48, v216, s42, v48\n"
    "v_dot2_f32_bf16 v49, v216, s43, v49\n"
    "v_dot2_f32_bf16 v50, v217, s42, v50\n"
    "v_dot2_f32_bf16 v51, v217, s43, v51\n"
    "v_dot2_f32_bf16 v52, v218, s42, v52\n"
    "v_dot2_f32_bf16 v53, v218, s43, v53\n"
    "v_dot2_f32_bf16 v54, v219, s42, v54\n"
    "v_dot2_f32_bf16 v55, v219, s43, v55\n"
    "v_dot2_f32_bf16 v48, v96, s42, v48\n"
    "v_dot2_f32_bf16 v49, v96, s43, v49\n"
    "v_dot2_f32_bf16 v50, v97, s42, v50\n"
    "v_dot2_f32_bf16 v51, v97, s43, v51\n"
    "v_dot2_f32_bf16 v52, v98, s42, v52\n"
    "v_dot2_f32_bf16 v53, v98, s43, v53\n"
    "v_dot2_f32_bf16 v54, v99, s42, v54\n"
    "v_dot2_f32_bf16 v55, v99, s43, v55\n"
    "s_nop 2\n"
    "v_cvt_pk_bf16_f32 v56, v48, v49\n"
    "v_cvt_pk_bf16_f32 v57, v50, v51\n"
    "v_cvt_pk_bf16_f32 v58, v52, v53\n"
    "v_cvt_pk_bf16_f32 v59, v54, v55\n"
    "buffer_store_dwordx2 v[56:57], v243, s[32:35], s38 offen offset:0 sc1\n"
    "buffer_store_dwordx2 v[58:59], v243, s[32:35], s39 offen offset:0 sc1\n"
    "buffer_load_dword v248, v242, s[28:31], s52 offen offset:0 sc1\n"
    "s_waitcnt vmcnt(0)\n"
    "v_cmp_ne_u32 vcc, v249, v248\n"
    "s_nop 3\n"
    "s_cbranch_vccnz L_r1_slow_%=\n"
    "L_r1_ret_%=:\n"
    "buffer_load_dwordx4 v[20:23], v237, s[28:31], s79 offen offset:0 sc1\n"
    "buffer_load_dwordx4 v[24:27], v237, s[28:31], s79 offen offset:1024 sc1\n"
    "buffer_load_dwordx4 v[28:31], v237, s[28:31], s79 offen offset:2048 sc1\n"
    "buffer_load_dwordx4 v[32:35], v237, s[28:31], s79 offen offset:3072 sc1\n"
    "buffer_load_dwordx4 v[36:39], v239, s[28:31], s79 offen offset:0 sc1\n"
    "buffer_load_dwordx4 v[40:43], v239, s[28:31], s79 offen offset:1024 sc1\n"
    "buffer_load_dwordx4 v[44:47], v239, s[28:31], s79 offen offset:2048 sc1\n"
    "s_waitcnt vmcnt(6)\n"
    "v_dot2_f32_bf16 v48, v20, s42, v12\n"
    "v_dot2_f32_bf16 v49, v20, s43, v13\n"
    "v_dot2_f32_bf16 v50, v21, s42, v14\n"
    "v_dot2_f32_bf16 v51, v21, s43, v15\n"
    "v_dot2_f32_bf16 v52, v22, s42, v16\n"
    "v_dot2_f32_bf16 v53, v22, s43, v17\n"
    "v_dot2_f32_bf16 v54, v23, s42, v18\n"
    "v_dot2_f32_bf16 v55, v23, s43, v19\n"
    "s_waitcnt vmcnt(5)\n"
    "v_dot2_f32_bf16 v48, v24, s42, v48\n"
    "v_dot2_f32_bf16 v49, v24, s43, v49\n"
    "v_dot2_f32_bf16 v50, v25, s42, v50\n"
    "v_dot2_f32_bf16 v51, v25, s43, v51\n"
    "v_dot2_f32_bf16 v52, v26, s42, v52\n"
    "v_dot2_f32_bf16 v53, v26, s43, v53\n"
    "v_dot2_f32_bf16 v54, v27, s42, v54\n"
    "v_dot2_f32_bf16 v55, v27, s43, v55\n"
    "s_waitcnt vmcnt(4)\n"
    "v_dot2_f32_bf16 v48, v28, s42, v48\n"
    "v_dot2_f32_bf16 v49, v28, s43, v49\n"
    "v_dot2_f32_bf16 v50, v29, s42, v50\n"
    "v_dot2_f32_bf16 v51, v29, s43, v51\n"
    "v_dot2_f32_bf16 v52, v30, s42, v52\n"
    "v_dot2_f32_bf16 v53, v30, s43, v53\n"
    "v_dot2_f32_bf16 v54, v31, s42, v54\n"
    "v_dot2_f32_bf16 v55, v31, s43, v55\n"
    "s_waitcnt vmcnt(3)\n"
    "v_dot2_f32_bf16 v48, v32, s42, v48\n"
    "v_dot2_f32_bf16 v49, v32, s43, v49\n"
    "v_dot2_f32_bf16 v50, v33, s42, v50\n"
    "v_dot2_f32_bf16 v51, v33, s43, v51\n"
    "v_dot2_f32_bf16 v52, v34, s42, v52\n"
    "v_dot2_f32_bf16 v53, v34, s43, v53\n"
    "v_dot2_f32_bf16 v54, v35, s42, v54\n"
    "v_dot2_f32_bf16 v55, v35, s43, v55\n"
    "s_waitcnt vmcnt(2)\n"
    "v_dot2_f32_bf16 v48, v36, s42, v48\n"
    "v_dot2_f32_bf16 v49, v36, s43, v49\n"
    "v_dot2_f32_bf16 v50, v37, s42, v50\n"
    "v_dot2_f32_bf16 v51, v37, s43, v51\n"
    "v_dot2_f32_bf16 v52, v38, s42, v52\n"
    "v_dot2_f32_bf16 v53, v38, s43, v53\n"
    "v_dot2_f32_bf16 v54, v39, s42, v54\n"
    "v_dot2_f32_bf16 v55, v39, s43, v55\n"
    "s_waitcnt vmcnt(1)\n"
    "v_dot2_f32_bf16 v48, v40, s42, v48\n"
    "v_dot2_f32_bf16 v49, v40, s43, v49\n"
    "v_dot2_f32_bf16 v50, v41, s42, v50\n"
    "v_dot2_f32_bf16 v51, v41, s43, v51\n"
    "v_dot2_f32_bf16 v52, v42, s42, v52\n"
    "v_dot2_f32_bf16 v53, v42, s43, v53\n"
    "v_dot2_f32_bf16 v54, v43, s42, v54\n"
    "v_dot2_f32_bf16 v55, v43, s43, v55\n"
    "s_waitcnt vmcnt(0)\n"
    "v_dot2_f32_bf16 v48, v44, s42, v48\n"
    "v_dot2_f32_bf16 v49, v44, s43, v49\n"
    "v_dot2_f32_bf16 v50, v45, s42, v50\n"
    "v_dot2_f32_bf16 v51, v45, s43, v51\n"
    "v_dot2_f32_bf16 v52, v46, s42, v52\n"
    "v_dot2_f32_bf16 v53, v46, s43, v53\n"
    "v_dot2_f32_bf16 v54, v47, s42, v54\n"
    "v_dot2_f32_bf16 v55, v47, s43, v55\n"
    "s_nop 2\n"
    "v_cvt_pk_bf16_f32 v56, v48, v49\n"
    "v_cvt_pk_bf16_f32 v57, v50, v51\n"
    "v_cvt_pk_bf16_f32 v58, v52, v53\n"
    "v_cvt_pk_bf16_f32 v59, v54, v55\n"
    "buffer_store_dwordx2 v[56:57], v243, s[32:35], s40 offen offset:0 sc1\n"
    "buffer_store_dwordx2 v[58:59], v243, s[32:35], s41 offen offset:0 sc1\n"
    "L_tail_%=:\n"
    "s_cmp_lg_u32 s59, 17920\n"
    "s_cbranch_scc1 L_done_%=\n"
    "v_lshlrev_b32 v254, 7, v237\n"
    "v_add_u32 v254, 4096, v254\n"
    "s_mov_b32 s82, 0\n"
    "L_bump_%=:\n"
    "buffer_load_dword v4, v254, s[28:31], 0 offen sc1\n"
    "buffer_load_dword v5, v254, s[28:31], 0 offen offset:512 sc1\n"
    "buffer_load_dword v6, v254, s[28:31], 0 offen offset:1024 sc1\n"
    "buffer_load_dword v7, v254, s[28:31], 0 offen offset:1536 sc1\n"
    "s_waitcnt vmcnt(0)\n"
    "v_xor_b32 v4, v249, v4\n"
    "v_xor_b32 v5, v249, v5\n"
    "v_xor_b32 v6, v249, v6\n"
    "v_xor_b32 v7, v249, v7\n"
    "v_or3_b32 v4, v4, v5, v6\n"
    "v_or_b32 v4, v4, v7\n"
    "v_cmp_ne_u32 vcc, 0, v4\n"
    "s_nop 4\n"
    "s_cbranch_vccz L_bump_go_%=\n"
    "s_sleep 4\n"
    "s_add_u32 s82, s82, 1\n"
    "s_cmp_lt_u32 s82, 200000\n"
    "s_cbranch_scc1 L_bump_%=\n"
    "L_bump_go_%=:\n"
    "buffer_store_dword v249, v240, s[28:31], 0 offen sc1\n"
    "L_done_%=:\n"
    "s_endpgm\n"
    "L_alt_%=:\n"
    "s_mov_b32 s80, 1040356334\n"
    "s_and_b32 s88, s61, 1\n"
    "ds_read_b64_tr_b16 a[108:109], v198 offset:32768\n"
    "v_mfma_f32_16x16x32_bf16 v[20:23], a[120:123], a[184:187], v[20:23]\n"
    "s_lshl_b32 s98, s88, 7\n"
    "ds_read_b64_tr_b16 a[110:111], v198 offset:33792\n"
    "buffer_load_dwordx4 v185, s[8:11], s92 offen offset:1024 lds\n"
    "v_mfma_f32_16x16x32_bf16 v[24:27], a[120:123], a[188:191], v[24:27]\n"
    "s_waitcnt lgkmcnt(4)\n"
    "s_xor_b32 s99, s98, 128\n"
    "s_mul_i32 s46, s88, 73728\n"
    "v_mfma_f32_16x16x32_bf16 v[28:31], a[124:127], a[180:183], v[28:31]\n"
    "s_sub_u32 s47, 73728, s46\n"
    "s_mul_i32 s50, s53, 5120\n"
    "ds_read_b64_tr_b16 a[112:113], v199 offset:32768\n"
    "v_mfma_f32_16x16x32_bf16 v[32:35], a[124:127], a[184:187], v[32:35]\n"
    "s_and_b32 s97, s55, 3\n"
    "s_lshl3_add_u32 s96, s61, s97\n"
    "ds_read_b64_tr_b16 a[114:115], v199 offset:33792\n"
    "v_mfma_f32_16x16x32_bf16 v[36:39], a[124:127], a[188:191], v[36:39]\n"
    "s_waitcnt lgkmcnt(4)\n"
    "s_lshl_b32 s96, s96, 3\n"
    "s_lshl2_add_u32 s96, s54, s96\n"
    "v_mfma_f32_16x16x32_bf16 v[40:43], a[128:131], a[180:183], v[40:43]\n"
    "s_lshl2_add_u32 s96, s96, s53\n"
    "ds_read_b64_tr_b16 a[116:117], v200 offset:32768\n"
    "buffer_load_dwordx4 v186, s[8:11], s92 offen offset:2048 lds\n"
    "v_mfma_f32_16x16x32_bf16 v[44:47], a[128:131], a[184:187], v[44:47]\n"
    "s_lshl_b32 s96, s96, 7\n"
    "s_add_u32 s96, s96, 135168\n"
    "ds_read_b64_tr_b16 a[118:119], v200 offset:33792\n"
    "v_mfma_f32_16x16x32_bf16 v[48:51], a[128:131], a[188:191], v[48:51]\n"
    "s_waitcnt lgkmcnt(4)\n"
    "v_and_b32 v207, 1, v202\n"
    "v_bfe_u32 v208, v202, 1, 2\n"
    "v_mfma_f32_16x16x32_bf16 v[52:55], a[108:111], a[180:183], v[52:55]\n"
    "s_add_u32 m0, s60, 3072\n"
    "ds_read_b64_tr_b16 a[120:121], v201 offset:32768\n"
    "v_lshlrev_b32 v207, 14, v207\n"
    "v_mfma_f32_16x16x32_bf16 v[56:59], a[108:111], a[184:187], v[56:59]\n"
    "ds_read_b64_tr_b16 a[122:123], v201 offset:33792\n"
    "buffer_load_dwordx4 v187, s[8:11], s92 offen offset:0 lds\n"
    "v_mfma_f32_16x16x32_bf16 v[60:63], a[108:111], a[188:191], v[60:63]\n"
    "s_waitcnt lgkmcnt(4)\n"
    "v_lshl_add_u32 v207, v208, 9, v207\n"
    "v_add_u32 v242, s96, v207\n"
    "v_mfma_f32_16x16x32_bf16 v[64:67], a[112:115], a[180:183], v[64:67]\n"
    "s_movk_i32 s96, 2304\n"
    "ds_read_b64_tr_b16 a[124:125], v194 offset:40960\n"
    "v_lshlrev_b32 v207, 3, v204\n"
    "v_mfma_f32_16x16x32_bf16 v[68:71], a[112:115], a[184:187], v[68:71]\n"
    "ds_read_b64_tr_b16 a[126:127], v194 offset:41984\n"
    "v_mad_u32_u24 v243, v203, s96, v207\n"
    "v_mfma_f32_16x16x32_bf16 v[72:75], a[112:115], a[188:191], v[72:75]\n"
    "s_waitcnt lgkmcnt(4)\n"
    "s_movk_i32 s96, 3277\n"
    "s_movk_i32 s97, 1152\n"
    "v_mfma_f32_16x16x32_bf16 v[76:79], a[116:119], a[180:183], v[76:79]\n"
    "s_mul_i32 s81, s53, 18432\n"
    "ds_read_b64_tr_b16 a[128:129], v195 offset:40960\n"
    "buffer_load_dwordx4 v188, s[8:11], s92 offen offset:1024 lds\n"
    "v_mfma_f32_16x16x32_bf16 v[80:83], a[116:119], a[184:187], v[80:83]\n"
    "ds_read_b64_tr_b16 a[130:131], v195 offset:41984\n"
    "v_add_u32 v207, 0, v202\n"
    "v_mfma_f32_16x16x32_bf16 v[84:87], a[116:119], a[188:191], v[84:87]\n"
    "s_waitcnt lgkmcnt(4)\n"
    "v_mul_u32_u24 v208, s96, v207\n"
    "v_lshrrev_b32 v208, 16, v208\n"
    "v_mfma_f32_16x16x32_bf16 v[88:91], a[120:123], a[180:183], v[88:91]\n"
    "ds_read_b64_tr_b16 a[108:109], v196 offset:40960\n"
    "v_mul_u32_u24 v226, 20, v208\n"
    "v_mfma_f32_16x16x32_bf16 v[92:95], a[120:123], a[184:187], v[92:95]\n"
    "ds_read_b64_tr_b16 a[110:111], v196 offset:41984\n"
    "buffer_load_dwordx4 v189, s[8:11], s92 offen offset:2048 lds\n"
    "v_mfma_f32_16x16x32_bf16 v[96:99], a[120:123], a[188:191], v[96:99]\n"
    "s_waitcnt lgkmcnt(4)\n"
    "v_lshrrev_b32 v209, 2, v208\n"
    "v_sub_u32 v207, v207, v226\n"
    "v_mfma_f32_16x16x32_bf16 v[4:7], a[124:127], a[192:195], v[4:7]\n"
    "ds_read_b64_tr_b16 a[112:113], v197 offset:40960\n"
    "v_and_b32 v209, 2, v209\n"
    "v_mfma_f32_16x16x32_bf16 v[8:11], a[124:127], a[196:199], v[8:11]\n"
    "ds_read_b64_tr_b16 a[114:115], v197 offset:41984\n"
    "v_xor_b32 v207, v207, v209\n"
    "v_mfma_f32_16x16x32_bf16 v[12:15], a[124:127], a[200:203], v[12:15]\n"
    "s_waitcnt lgkmcnt(4)\n"
    "v_lshlrev_b32 v207, 4, v207\n"
    "v_mad_u32_u24 v207, v208, s97, v207\n"
    "v_mfma_f32_16x16x32_bf16 v[16:19], a[128:131], a[192:195], v[16:19]\n"
    "s_waitcnt vmcnt(10)\n"
    "ds_read_b64_tr_b16 a[116:117], v198 offset:40960\n"
    "ds_read_b128 a[156:159], v231 offset:6144\n"
    "v_mfma_f32_16x16x32_bf16 v[20:23], a[128:131], a[196:199], v[20:23]\n"
    "ds_read_b64_tr_b16 a[118:119], v198 offset:41984\n"
    "ds_read_b128 a[160:163], v231 offset:8192\n"
    "v_mfma_f32_16x16x32_bf16 v[24:27], a[128:131], a[200:203], v[24:27]\n"
    "s_waitcnt lgkmcnt(6)\n"
    "v_add_u32 v207, s81, v207\n"
    "v_subrev_u32 v226, 0, v207\n"
    "v_mfma_f32_16x16x32_bf16 v[28:31], a[108:111], a[192:195], v[28:31]\n"
    "ds_read_b64_tr_b16 a[120:121], v199 offset:40960\n"
    "ds_read_b128 a[164:167], v231 offset:10240\n"
    "v_mfma_f32_16x16x32_bf16 v[32:35], a[108:111], a[196:199], v[32:35]\n"
    "ds_read_b64_tr_b16 a[122:123], v199 offset:41984\n"
    "v_add_u32 v207, 64, v202\n"
    "v_mfma_f32_16x16x32_bf16 v[36:39], a[108:111], a[200:203], v[36:39]\n"
    "s_waitcnt lgkmcnt(7)\n"
    "v_mul_u32_u24 v208, s96, v207\n"
    "v_lshrrev_b32 v208, 16, v208\n"
    "v_mfma_f32_16x16x32_bf16 v[40:43], a[112:115], a[192:195], v[40:43]\n"
    "ds_read_b64_tr_b16 a[124:125], v200 offset:40960\n"
    "ds_read_b128 a[168:171], v232 offset:6144\n"
    "v_mfma_f32_16x16x32_bf16 v[44:47], a[112:115], a[196:199], v[44:47]\n"
    "ds_read_b64_tr_b16 a[126:127], v200 offset:41984\n"
    "v_mul_u32_u24 v227, 20, v208\n"
    "v_mfma_f32_16x16x32_bf16 v[48:51], a[112:115], a[200:203], v[48:51]\n"
    "s_waitcnt lgkmcnt(7)\n"
    "v_lshrrev_b32 v209, 2, v208\n"
    "v_sub_u32 v207, v207, v227\n"
    "v_mfma_f32_16x16x32_bf16 v[52:55], a[116:119], a[192:195], v[52:55]\n"
    "ds_read_b64_tr_b16 a[128:129], v201 offset:40960\n"
    "ds_read_b128 a[172:175], v232 offset:8192\n"
    "v_mfma_f32_16x16x32_bf16 v[56:59], a[116:119], a[196:199], v[56:59]\n"
    "ds_read_b64_tr_b16 a[130:131], v201 offset:41984\n"
    "s_barrier\n"
    "v_mfma_f32_16x16x32_bf16 v[60:63], a[116:119], a[200:203], v[60:63]\n"
    "s_waitcnt lgkmcnt(6)\n"
    "v_and_b32 v209, 2, v209\n"
    "v_xor_b32 v207, v207, v209\n"
    "v_mfma_f32_16x16x32_bf16 v[64:67], a[120:123], a[192:195], v[64:67]\n"
    "s_lshl_b32 s62, s92, 12\n"
    "ds_read_b64_tr_b16 a[108:109], v194 offset:49152\n"
    "ds_read_b128 a[176:179], v232 offset:10240\n"
    "v_mfma_f32_16x16x32_bf16 v[68:71], a[120:123], a[196:199], v[68:71]\n"
    "s_add_u32 m0, s7, 16384\n"
    "ds_read_b64_tr_b16 a[110:111], v194 offset:50176\n"
    "buffer_load_dwordx4 v190, s[20:23], s62 offen offset:0 lds\n"
    "v_mfma_f32_16x16x32_bf16 v[72:75], a[120:123], a[200:203], v[72:75]\n"
    "s_waitcnt lgkmcnt(6)\n"
    "buffer_load_dwordx4 v191, s[20:23], s62 offen offset:1024 lds\n"
    "v_lshlrev_b32 v207, 4, v207\n"
    "v_mfma_f32_16x16x32_bf16 v[76:79], a[124:127], a[192:195], v[76:79]\n"
    "ds_read_b64_tr_b16 a[112:113], v195 offset:49152\n"
    "v_mad_u32_u24 v207, v208, s97, v207\n"
    "v_mfma_f32_16x16x32_bf16 v[80:83], a[124:127], a[196:199], v[80:83]\n"
    "ds_read_b64_tr_b16 a[114:115], v195 offset:50176\n"
    "buffer_load_dwordx4 v192, s[20:23], s62 offen offset:2048 lds\n"
    "v_mfma_f32_16x16x32_bf16 v[84:87], a[124:127], a[200:203], v[84:87]\n"
    "s_waitcnt lgkmcnt(5)\n"
    "v_add_u32 v207, s81, v207\n"
    "v_subrev_u32 v227, 1024, v207\n"
    "v_mfma_f32_16x16x32_bf16 v[88:91], a[128:131], a[192:195], v[88:91]\n"
    "ds_read_b64_tr_b16 a[116:117], v196 offset:49152\n"
    "buffer_load_dwordx4 v193, s[20:23], s62 offen offset:3072 lds\n"
    "v_mfma_f32_16x16x32_bf16 v[92:95], a[128:131], a[196:199], v[92:95]\n"
    "ds_read_b64_tr_b16 a[118:119], v196 offset:50176\n"
    "v_add_u32 v207, 128, v202\n"
    "v_mfma_f32_16x16x32_bf16 v[96:99], a[128:131], a[200:203], v[96:99]\n"
    "s_waitcnt lgkmcnt(4)\n"
    "v_mul_u32_u24 v208, s96, v207\n"
    "v_lshrrev_b32 v208, 16, v208\n"
    "v_mfma_f32_16x16x32_bf16 v[4:7], a[108:111], a[132:135], v[4:7]\n"
    "ds_read_b64_tr_b16 a[120:121], v197 offset:49152\n"
    "v_mul_u32_u24 v228, 20, v208\n"
    "v_mfma_f32_16x16x32_bf16 v[8:11], a[108:111], a[136:139], v[8:11]\n"
    "ds_read_b64_tr_b16 a[122:123], v197 offset:50176\n"
    "v_lshrrev_b32 v209, 2, v208\n"
    "v_mfma_f32_16x16x32_bf16 v[12:15], a[108:111], a[140:143], v[12:15]\n"
    "s_waitcnt lgkmcnt(4)\n"
    "v_sub_u32 v207, v207, v228\n"
    "v_and_b32 v209, 2, v209\n"
    "v_mfma_f32_16x16x32_bf16 v[16:19], a[112:115], a[132:135], v[16:19]\n"
    "s_add_u32 m0, s60, 6144\n"
    "ds_read_b64_tr_b16 a[124:125], v198 offset:49152\n"
    "buffer_load_dwordx4 v184, s[8:11], s4 offen offset:0 lds\n"
    "v_mfma_f32_16x16x32_bf16 v[20:23], a[112:115], a[136:139], v[20:23]\n"
    "ds_read_b64_tr_b16 a[126:127], v198 offset:50176\n"
    "buffer_load_dwordx4 v185, s[8:11], s4 offen offset:1024 lds\n"
    "v_mfma_f32_16x16x32_bf16 v[24:27], a[112:115], a[140:143], v[24:27]\n"
    "s_waitcnt lgkmcnt(4)\n"
    "v_xor_b32 v207, v207, v209\n"
    "v_lshlrev_b32 v207, 4, v207\n"
    "v_mfma_f32_16x16x32_bf16 v[28:31], a[116:119], a[132:135], v[28:31]\n"
    "ds_read_b64_tr_b16 a[128:129], v199 offset:49152\n"
    "buffer_load_dwordx4 v186, s[8:11], s4 offen offset:2048 lds\n"
    "v_mfma_f32_16x16x32_bf16 v[32:35], a[116:119], a[136:139], v[32:35]\n"
    "s_add_u32 m0, s60, 9216\n"
    "ds_read_b64_tr_b16 a[130:131], v199 offset:50176\n"
    "v_mad_u32_u24 v207, v208, s97, v207\n"
    "v_mfma_f32_16x16x32_bf16 v[36:39], a[116:119], a[140:143], v[36:39]\n"
    "s_waitcnt lgkmcnt(4)\n"
    "v_add_u32 v207, s81, v207\n"
    "v_subrev_u32 v228, 2048, v207\n"
    "v_mfma_f32_16x16x32_bf16 v[40:43], a[120:123], a[132:135], v[40:43]\n"
    "ds_read_b64_tr_b16 a[108:109], v200 offset:49152\n"
    "buffer_load_dwordx4 v187, s[8:11], s4 offen offset:0 lds\n"
    "v_mfma_f32_16x16x32_bf16 v[44:47], a[120:123], a[136:139], v[44:47]\n"
    "ds_read_b64_tr_b16 a[110:111], v200 offset:50176\n"
    "v_add_u32 v207, 192, v202\n"
    "v_mfma_f32_16x16x32_bf16 v[48:51], a[120:123], a[140:143], v[48:51]\n"
    "s_waitcnt lgkmcnt(4)\n"
    "v_mul_u32_u24 v208, s96, v207\n"
    "v_lshrrev_b32 v208, 16, v208\n"
    "v_mfma_f32_16x16x32_bf16 v[52:55], a[124:127], a[132:135], v[52:55]\n"
    "ds_read_b64_tr_b16 a[112:113], v201 offset:49152\n"
    "buffer_load_dwordx4 v188, s[8:11], s4 offen offset:1024 lds\n"
    "v_mul_u32_u24 v229, 20, v208\n"
    "v_lshrrev_b32 v209, 2, v208\n"
    "v_sub_u32 v207, v207, v229\n"
    "v_and_b32 v209, 2, v209\n"
    "v_xor_b32 v207, v207, v209\n"
    "v_lshlrev_b32 v207, 4, v207\n"
    "v_mad_u32_u24 v207, v208, s97, v207\n"
    "v_add_u32 v207, s81, v207\n"
    "v_subrev_u32 v229, 0, v207\n"
    "v_add_u32 v207, 256, v202\n"
    "v_mul_u32_u24 v208, s96, v207\n"
    "v_lshrrev_b32 v208, 16, v208\n"
    "v_mul_u32_u24 v230, 20, v208\n"
    "v_lshrrev_b32 v209, 2, v208\n"
    "v_sub_u32 v207, v207, v230\n"
    "v_and_b32 v209, 2, v209\n"
    "v_xor_b32 v207, v207, v209\n"
    "v_lshlrev_b32 v207, 4, v207\n"
    "v_mad_u32_u24 v207, v208, s97, v207\n"
    "v_add_u32 v207, s81, v207\n"
    "v_subrev_u32 v230, 1024, v207\n"
    "v_mfma_f32_16x16x32_bf16 v[56:59], a[124:127], a[136:139], v[56:59]\n"
    "ds_read_b64_tr_b16 a[114:115], v201 offset:50176\n"
    "v_mfma_f32_16x16x32_bf16 v[60:63], a[124:127], a[140:143], v[60:63]\n"
    "s_waitcnt lgkmcnt(4)\n"
    "v_mfma_f32_16x16x32_bf16 v[64:67], a[128:131], a[132:135], v[64:67]\n"
    "ds_read_b64_tr_b16 a[116:117], v194 offset:57344\n"
    "buffer_load_dwordx4 v189, s[8:11], s4 offen offset:2048 lds\n"
    "v_mfma_f32_16x16x32_bf16 v[68:71], a[128:131], a[136:139], v[68:71]\n"
    "ds_read_b64_tr_b16 a[118:119], v194 offset:58368\n"
    "v_mfma_f32_16x16x32_bf16 v[72:75], a[128:131], a[140:143], v[72:75]\n"
    "s_waitcnt lgkmcnt(4)\n"
    "v_mfma_f32_16x16x32_bf16 v[76:79], a[108:111], a[132:135], v[76:79]\n"
    "s_add_u32 s63, s65, 0\n"
    "s_add_u32 m0, s60, 12288\n"
    "ds_read_b64_tr_b16 a[120:121], v195 offset:57344\n"
    "v_mfma_f32_16x16x32_bf16 v[80:83], a[108:111], a[136:139], v[80:83]\n"
    "ds_read_b64_tr_b16 a[122:123], v195 offset:58368\n"
    "buffer_load_dwordx4 v220, s[12:15], s63 offen offset:0 nt lds\n"
    "v_mfma_f32_16x16x32_bf16 v[84:87], a[108:111], a[140:143], v[84:87]\n"
    "s_waitcnt lgkmcnt(4)\n"
    "v_mfma_f32_16x16x32_bf16 v[88:91], a[112:115], a[132:135], v[88:91]\n"
    "ds_read_b64_tr_b16 a[124:125], v196 offset:57344\n"
    "buffer_load_dwordx4 v221, s[12:15], s63 offen offset:1024 nt lds\n"
    "v_mfma_f32_16x16x32_bf16 v[92:95], a[112:115], a[136:139], v[92:95]\n"
    "ds_read_b64_tr_b16 a[126:127], v196 offset:58368\n"
    "v_mfma_f32_16x16x32_bf16 v[96:99], a[112:115], a[140:143], v[96:99]\n"
    "s_waitcnt lgkmcnt(4)\n"
    "v_mfma_f32_16x16x32_bf16 v[4:7], a[116:119], a[144:147], v[4:7]\n"
    "ds_read_b64_tr_b16 a[128:129], v197 offset:57344\n"
    "buffer_load_dwordx4 v222, s[12:15], s63 offen offset:2048 nt lds\n"
    "v_mfma_f32_16x16x32_bf16 v[8:11], a[116:119], a[148:151], v[8:11]\n"
    "s_waitcnt vmcnt(13)\n"
    "ds_read_b64_tr_b16 a[130:131], v197 offset:58368\n"
    "v_mfma_f32_16x16x32_bf16 v[12:15], a[116:119], a[152:155], v[12:15]\n"
    "s_waitcnt lgkmcnt(4)\n"
    "s_add_u32 m0, s60, 15360\n"
    "ds_read_b128 a[180:183], v231 offset:0\n"
    "v_mfma_f32_16x16x32_bf16 v[16:19], a[120:123], a[144:147], v[16:19]\n"
    "ds_read_b64_tr_b16 a[108:109], v198 offset:57344\n"
    "ds_read_b128 a[184:187], v231 offset:2048\n"
    "v_mfma_f32_16x16x32_bf16 v[20:23], a[120:123], a[148:151], v[20:23]\n"
    "ds_read_b64_tr_b16 a[110:111], v198 offset:58368\n"
    "buffer_load_dwordx4 v223, s[12:15], s63 offen offset:0 nt lds\n"
    "v_mfma_f32_16x16x32_bf16 v[24:27], a[120:123], a[152:155], v[24:27]\n"
    "s_waitcnt lgkmcnt(6)\n"
    "v_mfma_f32_16x16x32_bf16 v[28:31], a[124:127], a[144:147], v[28:31]\n"
    "ds_read_b64_tr_b16 a[112:113], v199 offset:57344\n"
    "ds_read_b128 a[188:191], v231 offset:4096\n"
    "v_mfma_f32_16x16x32_bf16 v[32:35], a[124:127], a[148:151], v[32:35]\n"
    "ds_read_b64_tr_b16 a[114:115], v199 offset:58368\n"
    "buffer_load_dwordx4 v224, s[12:15], s63 offen offset:1024 nt lds\n"
    "v_mfma_f32_16x16x32_bf16 v[36:39], a[124:127], a[152:155], v[36:39]\n"
    "s_waitcnt lgkmcnt(7)\n"
    "v_mfma_f32_16x16x32_bf16 v[40:43], a[128:131], a[144:147], v[40:43]\n"
    "ds_read_b64_tr_b16 a[116:117], v200 offset:57344\n"
    "ds_read_b128 a[192:195], v232 offset:0\n"
    "v_mfma_f32_16x16x32_bf16 v[44:47], a[128:131], a[148:151], v[44:47]\n"
    "ds_read_b64_tr_b16 a[118:119], v200 offset:58368\n"
    "buffer_load_dwordx4 v225, s[12:15], s63 offen offset:2048 nt lds\n"
    "v_mfma_f32_16x16x32_bf16 v[48:51], a[128:131], a[152:155], v[48:51]\n"
    "s_waitcnt lgkmcnt(6)\n"
    "v_mfma_f32_16x16x32_bf16 v[52:55], a[108:111], a[144:147], v[52:55]\n"
    "ds_read_b64_tr_b16 a[120:121], v201 offset:57344\n"
    "ds_read_b128 a[196:199], v232 offset:2048\n"
    "v_mfma_f32_16x16x32_bf16 v[56:59], a[108:111], a[148:151], v[56:59]\n"
    "ds_read_b64_tr_b16 a[122:123], v201 offset:58368\n"
    "s_barrier\n"
    "v_mfma_f32_16x16x32_bf16 v[60:63], a[108:111], a[152:155], v[60:63]\n"
    "s_waitcnt lgkmcnt(6)\n"
    "s_lshl_b32 s62, s4, 12\n"
    "s_add_u32 m0, s7, 32768\n"
    "v_mfma_f32_16x16x32_bf16 v[64:67], a[112:115], a[144:147], v[64:67]\n"
    "ds_read_b64_tr_b16 a[124:125], v194 offset:0\n"
    "ds_read_b128 a[200:203], v232 offset:4096\n"
    "v_mfma_f32_16x16x32_bf16 v[68:71], a[112:115], a[148:151], v[68:71]\n"
    "ds_read_b64_tr_b16 a[126:127], v194 offset:1024\n"
    "buffer_load_dwordx4 v190, s[20:23], s62 offen offset:0 lds\n"
    "v_mfma_f32_16x16x32_bf16 v[72:75], a[112:115], a[152:155], v[72:75]\n"
    "s_waitcnt lgkmcnt(6)\n"
    "buffer_load_dwordx4 v191, s[20:23], s62 offen offset:1024 lds\n"
    "v_mfma_f32_16x16x32_bf16 v[76:79], a[116:119], a[144:147], v[76:79]\n"
    "ds_read_b64_tr_b16 a[128:129], v195 offset:0\n"
    "v_mfma_f32_16x16x32_bf16 v[80:83], a[116:119], a[148:151], v[80:83]\n"
    "ds_read_b64_tr_b16 a[130:131], v195 offset:1024\n"
    "buffer_load_dwordx4 v192, s[20:23], s62 offen offset:2048 lds\n"
    "v_mfma_f32_16x16x32_bf16 v[84:87], a[116:119], a[152:155], v[84:87]\n"
    "s_waitcnt lgkmcnt(5)\n"
    "v_mfma_f32_16x16x32_bf16 v[88:91], a[120:123], a[144:147], v[88:91]\n"
    "ds_read_b64_tr_b16 a[108:109], v196 offset:0\n"
    "buffer_load_dwordx4 v193, s[20:23], s62 offen offset:3072 lds\n"
    "v_mfma_f32_16x16x32_bf16 v[92:95], a[120:123], a[148:151], v[92:95]\n"
    "ds_read_b64_tr_b16 a[110:111], v196 offset:1024\n"
    "v_mfma_f32_16x16x32_bf16 v[96:99], a[120:123], a[152:155], v[96:99]\n"
    "s_waitcnt lgkmcnt(4)\n"
    "v_mfma_f32_16x16x32_bf16 v[4:7], a[124:127], a[156:159], v[4:7]\n"
    "ds_read_b64_tr_b16 a[112:113], v197 offset:0\n"
    "v_add_u32 v249, 1, v249\n"
    "v_mfma_f32_16x16x32_bf16 v[8:11], a[124:127], a[160:163], v[8:11]\n"
    "s_add_u32 m0, s60, 0\n"
    "ds_read_b64_tr_b16 a[114:115], v197 offset:1024\n"
    "v_mfma_f32_16x16x32_bf16 v[12:15], a[124:127], a[164:167], v[12:15]\n"
    "s_waitcnt lgkmcnt(4)\n"
    "buffer_load_dwordx4 v184, s[8:11], s5 offen offset:0 lds\n"
    "v_mfma_f32_16x16x32_bf16 v[16:19], a[128:131], a[156:159], v[16:19]\n"
    "ds_read_b64_tr_b16 a[116:117], v198 offset:0\n"
    "v_mfma_f32_16x16x32_bf16 v[20:23], a[128:131], a[160:163], v[20:23]\n"
    "ds_read_b64_tr_b16 a[118:119], v198 offset:1024\n"
    "buffer_load_dwordx4 v185, s[8:11], s5 offen offset:1024 lds\n"
    "v_mfma_f32_16x16x32_bf16 v[24:27], a[128:131], a[164:167], v[24:27]\n"
    "s_waitcnt lgkmcnt(4)\n"
    "v_mfma_f32_16x16x32_bf16 v[28:31], a[108:111], a[156:159], v[28:31]\n"
    "ds_read_b64_tr_b16 a[120:121], v199 offset:0\n"
    "v_mfma_f32_16x16x32_bf16 v[32:35], a[108:111], a[160:163], v[32:35]\n"
    "ds_read_b64_tr_b16 a[122:123], v199 offset:1024\n"
    "v_mfma_f32_16x16x32_bf16 v[36:39], a[108:111], a[164:167], v[36:39]\n"
    "s_waitcnt lgkmcnt(4)\n"
    "v_mfma_f32_16x16x32_bf16 v[40:43], a[112:115], a[156:159], v[40:43]\n"
    "ds_read_b64_tr_b16 a[124:125], v200 offset:0\n"
    "buffer_load_dwordx4 v186, s[8:11], s5 offen offset:2048 lds\n"
    "v_mfma_f32_16x16x32_bf16 v[44:47], a[112:115], a[160:163], v[44:47]\n"
    "ds_read_b64_tr_b16 a[126:127], v200 offset:1024\n"
    "v_mfma_f32_16x16x32_bf16 v[48:51], a[112:115], a[164:167], v[48:51]\n"
    "s_waitcnt lgkmcnt(4)\n"
    "s_add_u32 m0, s60, 3072\n"
    "v_mfma_f32_16x16x32_bf16 v[52:55], a[116:119], a[156:159], v[52:55]\n"
    "ds_read_b64_tr_b16 a[128:129], v201 offset:0\n"
    "v_mfma_f32_16x16x32_bf16 v[56:59], a[116:119], a[160:163], v[56:59]\n"
    "ds_read_b64_tr_b16 a[130:131], v201 offset:1024\n"
    "buffer_load_dwordx4 v187, s[8:11], s5 offen offset:0 lds\n"
    "v_mfma_f32_16x16x32_bf16 v[60:63], a[116:119], a[164:167], v[60:63]\n"
    "s_waitcnt lgkmcnt(4)\n"
    "v_mfma_f32_16x16x32_bf16 v[64:67], a[120:123], a[156:159], v[64:67]\n"
    "ds_read_b64_tr_b16 a[108:109], v194 offset:8192\n"
    "v_mfma_f32_16x16x32_bf16 v[68:71], a[120:123], a[160:163], v[68:71]\n"
    "ds_read_b64_tr_b16 a[110:111], v194 offset:9216\n"
    "v_mfma_f32_16x16x32_bf16 v[72:75], a[120:123], a[164:167], v[72:75]\n"
    "s_waitcnt lgkmcnt(4)\n"
    "v_mfma_f32_16x16x32_bf16 v[76:79], a[124:127], a[156:159], v[76:79]\n"
    "ds_read_b64_tr_b16 a[112:113], v195 offset:8192\n"
    "buffer_load_dwordx4 v188, s[8:11], s5 offen offset:1024 lds\n"
    "v_mfma_f32_16x16x32_bf16 v[80:83], a[124:127], a[160:163], v[80:83]\n"
    "ds_read_b64_tr_b16 a[114:115], v195 offset:9216\n"
    "v_mfma_f32_16x16x32_bf16 v[84:87], a[124:127], a[164:167], v[84:87]\n"
    "s_waitcnt lgkmcnt(4)\n"
    "v_mfma_f32_16x16x32_bf16 v[88:91], a[128:131], a[156:159], v[88:91]\n"
    "ds_read_b64_tr_b16 a[116:117], v196 offset:8192\n"
    "v_mfma_f32_16x16x32_bf16 v[92:95], a[128:131], a[160:163], v[92:95]\n"
    "ds_read_b64_tr_b16 a[118:119], v196 offset:9216\n"
    "buffer_load_dwordx4 v189, s[8:11], s5 offen offset:2048 lds\n"
    "v_mfma_f32_16x16x32_bf16 v[96:99], a[128:131], a[164:167], v[96:99]\n"
    "s_waitcnt lgkmcnt(4)\n"
    "v_mfma_f32_16x16x32_bf16 v[4:7], a[108:111], a[168:171], v[4:7]\n"
    "s_waitcnt vmcnt(16)\n"
    "ds_read_b64_tr_b16 a[120:121], v197 offset:8192\n"
    "v_mfma_f32_16x16x32_bf16 v[8:11], a[108:111], a[172:175], v[8:11]\n"
    "ds_read_b64_tr_b16 a[122:123], v197 offset:9216\n"
    "ds_read_b128 a[132:135], v231 offset:6144\n"
    "v_mfma_f32_16x16x32_bf16 v[12:15], a[108:111], a[176:179], v[12:15]\n"
    "s_waitcnt lgkmcnt(5)\n"
    "v_mfma_f32_16x16x32_bf16 v[16:19], a[112:115], a[168:171], v[16:19]\n"
    "ds_read_b64_tr_b16 a[124:125], v198 offset:8192\n"
    "ds_read_b128 a[136:139], v231 offset:8192\n"
    "v_mfma_f32_16x16x32_bf16 v[20:23], a[112:115], a[172:175], v[20:23]\n"
    "ds_read_b64_tr_b16 a[126:127], v198 offset:9216\n"
    "v_mfma_f32_16x16x32_bf16 v[24:27], a[112:115], a[176:179], v[24:27]\n"
    "s_waitcnt lgkmcnt(6)\n"
    "v_mfma_f32_16x16x32_bf16 v[28:31], a[116:119], a[168:171], v[28:31]\n"
    "ds_read_b64_tr_b16 a[128:129], v199 offset:8192\n"
    "ds_read_b128 a[140:143], v231 offset:10240\n"
    "v_mfma_f32_16x16x32_bf16 v[32:35], a[116:119], a[172:175], v[32:35]\n"
    "ds_read_b64_tr_b16 a[130:131], v199 offset:9216\n"
    "v_mfma_f32_16x16x32_bf16 v[36:39], a[116:119], a[176:179], v[36:39]\n"
    "s_waitcnt lgkmcnt(7)\n"
    "v_mfma_f32_16x16x32_bf16 v[40:43], a[120:123], a[168:171], v[40:43]\n"
    "ds_read_b64_tr_b16 a[108:109], v200 offset:8192\n"
    "ds_read_b128 a[144:147], v232 offset:6144\n"
    "v_mfma_f32_16x16x32_bf16 v[44:47], a[120:123], a[172:175], v[44:47]\n"
    "ds_read_b64_tr_b16 a[110:111], v200 offset:9216\n"
    "v_mfma_f32_16x16x32_bf16 v[48:51], a[120:123], a[176:179], v[48:51]\n"
    "s_waitcnt lgkmcnt(6)\n"
    "v_mfma_f32_16x16x32_bf16 v[52:55], a[124:127], a[168:171], v[52:55]\n"
    "ds_read_b64_tr_b16 a[112:113], v201 offset:8192\n"
    "ds_read_b128 a[148:151], v232 offset:8192\n"
    "v_mfma_f32_16x16x32_bf16 v[56:59], a[124:127], a[172:175], v[56:59]\n"
    "ds_read_b64_tr_b16 a[114:115], v201 offset:9216\n"
    "s_barrier\n"
    "v_mfma_f32_16x16x32_bf16 v[60:63], a[124:127], a[176:179], v[60:63]\n"
    "s_waitcnt lgkmcnt(6)\n"
    "s_lshl_b32 s62, s5, 12\n"
    "s_add_u32 m0, s7, 49152\n"
    "v_mfma_f32_16x16x32_bf16 v[64:67], a[128:131], a[168:171], v[64:67]\n"
    "ds_read_b64_tr_b16 a[116:117], v194 offset:16384\n"
    "ds_read_b128 a[152:155], v232 offset:10240\n"
    "v_mfma_f32_16x16x32_bf16 v[68:71], a[128:131], a[172:175], v[68:71]\n"
    "ds_read_b64_tr_b16 a[118:119], v194 offset:17408\n"
    "buffer_load_dwordx4 v190, s[20:23], s62 offen offset:0 lds\n"
    "v_mfma_f32_16x16x32_bf16 v[72:75], a[128:131], a[176:179], v[72:75]\n"
    "s_waitcnt lgkmcnt(6)\n"
    "buffer_load_dwordx4 v191, s[20:23], s62 offen offset:1024 lds\n"
    "v_mfma_f32_16x16x32_bf16 v[76:79], a[108:111], a[168:171], v[76:79]\n"
    "ds_read_b64_tr_b16 a[120:121], v195 offset:16384\n"
    "v_mfma_f32_16x16x32_bf16 v[80:83], a[108:111], a[172:175], v[80:83]\n"
    "ds_read_b64_tr_b16 a[122:123], v195 offset:17408\n"
    "buffer_load_dwordx4 v192, s[20:23], s62 offen offset:2048 lds\n"
    "v_mfma_f32_16x16x32_bf16 v[84:87], a[108:111], a[176:179], v[84:87]\n"
    "s_waitcnt lgkmcnt(5)\n"
    "v_mfma_f32_16x16x32_bf16 v[88:91], a[112:115], a[168:171], v[88:91]\n"
    "ds_read_b64_tr_b16 a[124:125], v196 offset:16384\n"
    "buffer_load_dwordx4 v193, s[20:23], s62 offen offset:3072 lds\n"
    "v_mfma_f32_16x16x32_bf16 v[92:95], a[112:115], a[172:175], v[92:95]\n"
    "ds_read_b64_tr_b16 a[126:127], v196 offset:17408\n"
    "v_mfma_f32_16x16x32_bf16 v[96:99], a[112:115], a[176:179], v[96:99]\n"
    "s_waitcnt lgkmcnt(4)\n"
    "v_mfma_f32_16x16x32_bf16 v[4:7], a[116:119], a[180:183], v[4:7]\n"
    "s_waitcnt vmcnt(14)\n"
    "ds_read_b64_tr_b16 a[128:129], v197 offset:16384\n"
    "v_mfma_f32_16x16x32_bf16 v[8:11], a[116:119], a[184:187], v[8:11]\n"
    "ds_read_b64_tr_b16 a[130:131], v197 offset:17408\n"
    "ds_read_b64 a[204:205], v250 offset:12288\n"
    "v_mfma_f32_16x16x32_bf16 v[12:15], a[116:119], a[188:191], v[12:15]\n"
    "s_waitcnt lgkmcnt(5)\n"
    "s_add_u32 m0, s60, 6144\n"
    "ds_read_b64 a[206:207], v251 offset:12288\n"
    "v_mfma_f32_16x16x32_bf16 v[16:19], a[120:123], a[180:183], v[16:19]\n"
    "ds_read_b64_tr_b16 a[108:109], v198 offset:16384\n"
    "ds_read_b64 a[208:209], v250 offset:14336\n"
    "v_mfma_f32_16x16x32_bf16 v[20:23], a[120:123], a[184:187], v[20:23]\n"
    "ds_read_b64_tr_b16 a[110:111], v198 offset:17408\n"
    "ds_read_b64 a[210:211], v251 offset:14336\n"
    "v_mfma_f32_16x16x32_bf16 v[24:27], a[120:123], a[188:191], v[24:27]\n"
    "s_waitcnt lgkmcnt(8)\n"
    "buffer_load_dwordx4 v184, s[8:11], s6 offen offset:0 lds\n"
    "buffer_load_dwordx4 v185, s[8:11], s6 offen offset:1024 lds\n"
    "v_mfma_f32_16x16x32_bf16 v[28:31], a[124:127], a[180:183], v[28:31]\n"
    "ds_read_b64_tr_b16 a[112:113], v199 offset:16384\n"
    "ds_read_b64 a[212:213], v250 offset:16384\n"
    "v_mfma_f32_16x16x32_bf16 v[32:35], a[124:127], a[184:187], v[32:35]\n"
    "ds_read_b64_tr_b16 a[114:115], v199 offset:17408\n"
    "ds_read_b64 a[214:215], v251 offset:16384\n"
    "v_mfma_f32_16x16x32_bf16 v[36:39], a[124:127], a[188:191], v[36:39]\n"
    "s_waitcnt lgkmcnt(10)\n"
    "buffer_load_dwordx4 v186, s[8:11], s6 offen offset:2048 lds\n"
    "v_mfma_f32_16x16x32_bf16 v[40:43], a[128:131], a[180:183], v[40:43]\n"
    "s_add_u32 m0, s60, 9216\n"
    "ds_read_b64_tr_b16 a[116:117], v200 offset:16384\n"
    "ds_read_b64 a[216:217], v252 offset:12288\n"
    "v_mfma_f32_16x16x32_bf16 v[44:47], a[128:131], a[184:187], v[44:47]\n"
    "ds_read_b64_tr_b16 a[118:119], v200 offset:17408\n"
    "ds_read_b64 a[218:219], v253 offset:12288\n"
    "v_mfma_f32_16x16x32_bf16 v[48:51], a[128:131], a[188:191], v[48:51]\n"
    "s_waitcnt lgkmcnt(9)\n"
    "buffer_load_dwordx4 v187, s[8:11], s6 offen offset:0 lds\n"
    "v_mfma_f32_16x16x32_bf16 v[52:55], a[108:111], a[180:183], v[52:55]\n"
    "ds_read_b64_tr_b16 a[120:121], v201 offset:16384\n"
    "ds_read_b64 a[220:221], v252 offset:14336\n"
    "v_mfma_f32_16x16x32_bf16 v[56:59], a[108:111], a[184:187], v[56:59]\n"
    "ds_read_b64_tr_b16 a[122:123], v201 offset:17408\n"
    "ds_read_b64 a[222:223], v253 offset:14336\n"
    "v_mfma_f32_16x16x32_bf16 v[60:63], a[108:111], a[188:191], v[60:63]\n"
    "s_waitcnt lgkmcnt(9)\n"
    "buffer_load_dwordx4 v188, s[8:11], s6 offen offset:1024 lds\n"
    "v_mfma_f32_16x16x32_bf16 v[64:67], a[112:115], a[180:183], v[64:67]\n"
    "ds_read_b64_tr_b16 a[124:125], v194 offset:24576\n"
    "ds_read_b64 a[224:225], v252 offset:16384\n"
    "v_mfma_f32_16x16x32_bf16 v[68:71], a[112:115], a[184:187], v[68:71]\n"
    "ds_read_b64_tr_b16 a[126:127], v194 offset:25600\n"
    "ds_read_b64 a[226:227], v253 offset:16384\n"
    "v_mfma_f32_16x16x32_bf16 v[72:75], a[112:115], a[188:191], v[72:75]\n"
    "s_waitcnt lgkmcnt(9)\n"
    "buffer_load_dwordx4 v189, s[8:11], s6 offen offset:2048 lds\n"
    "v_mfma_f32_16x16x32_bf16 v[76:79], a[116:119], a[180:183], v[76:79]\n"
    "s_waitcnt lgkmcnt(0)\n"
    "s_add_u32 s63, s65, 128\n"
    "s_add_u32 m0, s60, 12288\n"
    "ds_read_b64_tr_b16 a[128:129], v195 offset:24576\n"
    "v_mfma_f32_16x16x32_bf16 v[80:83], a[116:119], a[184:187], v[80:83]\n"
    "ds_read_b64_tr_b16 a[130:131], v195 offset:25600\n"
    "buffer_load_dwordx4 v220, s[12:15], s63 offen offset:0 nt lds\n"
    "v_mfma_f32_16x16x32_bf16 v[84:87], a[116:119], a[188:191], v[84:87]\n"
    "v_mfma_f32_16x16x32_bf16 v[88:91], a[120:123], a[180:183], v[88:91]\n"
    "ds_read_b64_tr_b16 a[108:109], v196 offset:24576\n"
    "buffer_load_dwordx4 v221, s[12:15], s63 offen offset:1024 nt lds\n"
    "v_mfma_f32_16x16x32_bf16 v[92:95], a[120:123], a[184:187], v[92:95]\n"
    "ds_read_b64_tr_b16 a[110:111], v196 offset:25600\n"
    "v_mfma_f32_16x16x32_bf16 v[96:99], a[120:123], a[188:191], v[96:99]\n"
    "v_mfma_f32_16x16x32_bf16 v[4:7], a[124:127], a[192:195], v[4:7]\n"
    "ds_read_b64_tr_b16 a[112:113], v197 offset:24576\n"
    "buffer_load_dwordx4 v222, s[12:15], s63 offen offset:2048 nt lds\n"
    "v_mfma_f32_16x16x32_bf16 v[8:11], a[124:127], a[196:199], v[8:11]\n"
    "s_waitcnt vmcnt(13)\n"
    "ds_read_b64_tr_b16 a[114:115], v197 offset:25600\n"
    "v_mfma_f32_16x16x32_bf16 v[12:15], a[124:127], a[200:203], v[12:15]\n"
    "s_waitcnt lgkmcnt(4)\n"
    "s_add_u32 m0, s60, 15360\n"
    "ds_read_b128 a[156:159], v231 offset:0\n"
    "v_mfma_f32_16x16x32_bf16 v[16:19], a[128:131], a[192:195], v[16:19]\n"
    "ds_read_b64_tr_b16 a[116:117], v198 offset:24576\n"
    "ds_read_b128 a[160:163], v231 offset:2048\n"
    "v_mfma_f32_16x16x32_bf16 v[20:23], a[128:131], a[196:199], v[20:23]\n"
    "ds_read_b64_tr_b16 a[118:119], v198 offset:25600\n"
    "buffer_load_dwordx4 v223, s[12:15], s63 offen offset:0 nt lds\n"
    "v_mfma_f32_16x16x32_bf16 v[24:27], a[128:131], a[200:203], v[24:27]\n"
    "s_waitcnt lgkmcnt(6)\n"
    "v_mfma_f32_16x16x32_bf16 v[28:31], a[108:111], a[192:195], v[28:31]\n"
    "ds_read_b64_tr_b16 a[120:121], v199 offset:24576\n"
    "ds_read_b128 a[164:167], v231 offset:4096\n"
    "v_mfma_f32_16x16x32_bf16 v[32:35], a[108:111], a[196:199], v[32:35]\n"
    "ds_read_b64_tr_b16 a[122:123], v199 offset:25600\n"
    "buffer_load_dwordx4 v224, s[12:15], s63 offen offset:1024 nt lds\n"
    "v_mfma_f32_16x16x32_bf16 v[36:39], a[108:111], a[200:203], v[36:39]\n"
    "s_waitcnt lgkmcnt(7)\n"
    "v_mfma_f32_16x16x32_bf16 v[40:43], a[112:115], a[192:195], v[40:43]\n"
    "ds_read_b64_tr_b16 a[124:125], v200 offset:24576\n"
    "ds_read_b128 a[168:171], v232 offset:0\n"
    "v_mfma_f32_16x16x32_bf16 v[44:47], a[112:115], a[196:199], v[44:47]\n"
    "ds_read_b64_tr_b16 a[126:127], v200 offset:25600\n"
    "buffer_load_dwordx4 v225, s[12:15], s63 offen offset:2048 nt lds\n"
    "v_mfma_f32_16x16x32_bf16 v[48:51], a[112:115], a[200:203], v[48:51]\n"
    "s_waitcnt lgkmcnt(6)\n"
    "v_mfma_f32_16x16x32_bf16 v[52:55], a[116:119], a[192:195], v[52:55]\n"
    "ds_read_b64_tr_b16 a[128:129], v201 offset:24576\n"
    "ds_read_b128 a[172:175], v232 offset:2048\n"
    "v_mfma_f32_16x16x32_bf16 v[56:59], a[116:119], a[196:199], v[56:59]\n"
    "ds_read_b64_tr_b16 a[130:131], v201 offset:25600\n"
    "s_barrier\n"
    "v_mfma_f32_16x16x32_bf16 v[60:63], a[116:119], a[200:203], v[60:63]\n"
    "s_waitcnt lgkmcnt(6)\n"
    "s_lshl_b32 s62, s6, 12\n"
    "s_add_u32 m0, s7, 0\n"
    "v_mfma_f32_16x16x32_bf16 v[64:67], a[120:123], a[192:195], v[64:67]\n"
    "ds_read_b64_tr_b16 a[108:109], v194 offset:32768\n"
    "ds_read_b128 a[176:179], v232 offset:4096\n"
    "v_mfma_f32_16x16x32_bf16 v[68:71], a[120:123], a[196:199], v[68:71]\n"
    "ds_read_b64_tr_b16 a[110:111], v194 offset:33792\n"
    "buffer_load_dwordx4 v190, s[20:23], s62 offen offset:0 lds\n"
    "v_mfma_f32_16x16x32_bf16 v[72:75], a[120:123], a[200:203], v[72:75]\n"
    "s_waitcnt lgkmcnt(6)\n"
    "buffer_load_dwordx4 v191, s[20:23], s62 offen offset:1024 lds\n"
    "v_mfma_f32_16x16x32_bf16 v[76:79], a[124:127], a[192:195], v[76:79]\n"
    "ds_read_b64_tr_b16 a[112:113], v195 offset:32768\n"
    "v_mfma_f32_16x16x32_bf16 v[80:83], a[124:127], a[196:199], v[80:83]\n"
    "ds_read_b64_tr_b16 a[114:115], v195 offset:33792\n"
    "buffer_load_dwordx4 v192, s[20:23], s62 offen offset:2048 lds\n"
    "v_mfma_f32_16x16x32_bf16 v[84:87], a[124:127], a[200:203], v[84:87]\n"
    "s_waitcnt lgkmcnt(5)\n"
    "v_mfma_f32_16x16x32_bf16 v[88:91], a[128:131], a[192:195], v[88:91]\n"
    "ds_read_b64_tr_b16 a[116:117], v196 offset:32768\n"
    "buffer_load_dwordx4 v193, s[20:23], s62 offen offset:3072 lds\n"
    "v_mfma_f32_16x16x32_bf16 v[92:95], a[128:131], a[196:199], v[92:95]\n"
    "ds_read_b64_tr_b16 a[118:119], v196 offset:33792\n"
    "v_mfma_f32_16x16x32_bf16 v[96:99], a[128:131], a[200:203], v[96:99]\n"
    "s_waitcnt lgkmcnt(4)\n"
    "v_mfma_f32_16x16x32_bf16 v[4:7], a[108:111], a[132:135], v[4:7]\n"
    "ds_read_b64_tr_b16 a[120:121], v197 offset:32768\n"
    "v_mfma_f32_16x16x32_bf16 v[8:11], a[108:111], a[136:139], v[8:11]\n"
    "ds_read_b64_tr_b16 a[122:123], v197 offset:33792\n"
    "v_mfma_f32_16x16x32_bf16 v[12:15], a[108:111], a[140:143], v[12:15]\n"
    "s_waitcnt lgkmcnt(4)\n"
    "v_mfma_f32_16x16x32_bf16 v[16:19], a[112:115], a[132:135], v[16:19]\n"
    "ds_read_b64_tr_b16 a[124:125], v198 offset:32768\n"
    "v_mfma_f32_16x16x32_bf16 v[20:23], a[112:115], a[136:139], v[20:23]\n"
    "ds_read_b64_tr_b16 a[126:127], v198 offset:33792\n"
    "v_mfma_f32_16x16x32_bf16 v[24:27], a[112:115], a[140:143], v[24:27]\n"
    "s_waitcnt lgkmcnt(4)\n"
    "v_mfma_f32_16x16x32_bf16 v[28:31], a[116:119], a[132:135], v[28:31]\n"
    "ds_read_b64_tr_b16 a[128:129], v199 offset:32768\n"
    "v_mfma_f32_16x16x32_bf16 v[32:35], a[116:119], a[136:139], v[32:35]\n"
    "ds_read_b64_tr_b16 a[130:131], v199 offset:33792\n"
    "v_mfma_f32_16x16x32_bf16 v[36:39], a[116:119], a[140:143], v[36:39]\n"
    "s_waitcnt lgkmcnt(4)\n"
    "v_mfma_f32_16x16x32_bf16 v[40:43], a[120:123], a[132:135], v[40:43]\n"
    "ds_read_b64_tr_b16 a[108:109], v200 offset:32768\n"
    "v_mfma_f32_16x16x32_bf16 v[44:47], a[120:123], a[136:139], v[44:47]\n"
    "ds_read_b64_tr_b16 a[110:111], v200 offset:33792\n"
    "v_mfma_f32_16x16x32_bf16 v[48:51], a[120:123], a[140:143], v[48:51]\n"
    "s_waitcnt lgkmcnt(4)\n"
    "v_mfma_f32_16x16x32_bf16 v[52:55], a[124:127], a[132:135], v[52:55]\n"
    "ds_read_b64_tr_b16 a[112:113], v201 offset:32768\n"
    "v_mfma_f32_16x16x32_bf16 v[56:59], a[124:127], a[136:139], v[56:59]\n"
    "ds_read_b64_tr_b16 a[114:115], v201 offset:33792\n"
    "v_mfma_f32_16x16x32_bf16 v[60:63], a[124:127], a[140:143], v[60:63]\n"
    "s_waitcnt lgkmcnt(4)\n"
    "v_mfma_f32_16x16x32_bf16 v[64:67], a[128:131], a[132:135], v[64:67]\n"
    "ds_read_b64_tr_b16 a[116:117], v194 offset:40960\n"
    "v_mfma_f32_16x16x32_bf16 v[68:71], a[128:131], a[136:139], v[68:71]\n"
    "ds_read_b64_tr_b16 a[118:119], v194 offset:41984\n"
    "v_mfma_f32_16x16x32_bf16 v[72:75], a[128:131], a[140:143], v[72:75]\n"
    "s_waitcnt lgkmcnt(4)\n"
    "v_mfma_f32_16x16x32_bf16 v[76:79], a[108:111], a[132:135], v[76:79]\n"
    "s_add_u32 s64, s69, s46\n"
    "s_add_u32 m0, s50, 65536\n"
    "ds_read_b64_tr_b16 a[120:121], v195 offset:40960\n"
    "v_mfma_f32_16x16x32_bf16 v[80:83], a[108:111], a[136:139], v[80:83]\n"
    "ds_read_b64_tr_b16 a[122:123], v195 offset:41984\n"
    "buffer_load_dwordx4 v226, s[24:27], s64 offen offset:0 lds\n"
    "v_mfma_f32_16x16x32_bf16 v[84:87], a[108:111], a[140:143], v[84:87]\n"
    "s_waitcnt lgkmcnt(4)\n"
    "v_mfma_f32_16x16x32_bf16 v[88:91], a[112:115], a[132:135], v[88:91]\n"
    "ds_read_b64_tr_b16 a[124:125], v196 offset:40960\n"
    "buffer_load_dwordx4 v227, s[24:27], s64 offen offset:1024 lds\n"
    "v_mfma_f32_16x16x32_bf16 v[92:95], a[112:115], a[136:139], v[92:95]\n"
    "ds_read_b64_tr_b16 a[126:127], v196 offset:41984\n"
    "v_mfma_f32_16x16x32_bf16 v[96:99], a[112:115], a[140:143], v[96:99]\n"
    "s_waitcnt lgkmcnt(4)\n"
    "v_mfma_f32_16x16x32_bf16 v[4:7], a[116:119], a[144:147], v[4:7]\n"
    "s_waitcnt vmcnt(12)\n"
    "ds_read_b64_tr_b16 a[128:129], v197 offset:40960\n"
    "v_mfma_f32_16x16x32_bf16 v[8:11], a[116:119], a[148:151], v[8:11]\n"
    "ds_read_b64_tr_b16 a[130:131], v197 offset:41984\n"
    "ds_read_b128 a[180:183], v231 offset:6144\n"
    "v_mfma_f32_16x16x32_bf16 v[12:15], a[116:119], a[152:155], v[12:15]\n"
    "s_waitcnt lgkmcnt(5)\n"
    "buffer_load_dwordx4 v228, s[24:27], s64 offen offset:2048 lds\n"
    "v_mfma_f32_16x16x32_bf16 v[16:19], a[120:123], a[144:147], v[16:19]\n"
    "s_add_u32 m0, s50, 68608\n"
    "ds_read_b64_tr_b16 a[108:109], v198 offset:40960\n"
    "ds_read_b128 a[184:187], v231 offset:8192\n"
    "v_mfma_f32_16x16x32_bf16 v[20:23], a[120:123], a[148:151], v[20:23]\n"
    "ds_read_b64_tr_b16 a[110:111], v198 offset:41984\n"
    "v_mfma_f32_16x16x32_bf16 v[24:27], a[120:123], a[152:155], v[24:27]\n"
    "s_waitcnt lgkmcnt(6)\n"
    "buffer_load_dwordx4 v229, s[24:27], s64 offen offset:0 lds\n"
    "v_mfma_f32_16x16x32_bf16 v[28:31], a[124:127], a[144:147], v[28:31]\n"
    "ds_read_b64_tr_b16 a[112:113], v199 offset:40960\n"
    "ds_read_b128 a[188:191], v231 offset:10240\n"
    "v_mfma_f32_16x16x32_bf16 v[32:35], a[124:127], a[148:151], v[32:35]\n"
    "ds_read_b64_tr_b16 a[114:115], v199 offset:41984\n"
    "v_mfma_f32_16x16x32_bf16 v[36:39], a[124:127], a[152:155], v[36:39]\n"
    "s_waitcnt lgkmcnt(7)\n"
    "v_mfma_f32_16x16x32_bf16 v[40:43], a[128:131], a[144:147], v[40:43]\n"
    "ds_read_b64_tr_b16 a[116:117], v200 offset:40960\n"
    "ds_read_b128 a[192:195], v232 offset:6144\n"
    "v_mfma_f32_16x16x32_bf16 v[44:47], a[128:131], a[148:151], v[44:47]\n"
    "ds_read_b64_tr_b16 a[118:119], v200 offset:41984\n"
    "buffer_load_dwordx4 v230, s[24:27], s64 offen offset:1024 lds\n"
    "v_mfma_f32_16x16x32_bf16 v[48:51], a[128:131], a[152:155], v[48:51]\n"
    "s_waitcnt lgkmcnt(6)\n"
    "v_mfma_f32_16x16x32_bf16 v[52:55], a[108:111], a[144:147], v[52:55]\n"
    "ds_read_b64_tr_b16 a[120:121], v201 offset:40960\n"
    "ds_read_b128 a[196:199], v232 offset:8192\n"
    "v_mfma_f32_16x16x32_bf16 v[56:59], a[108:111], a[148:151], v[56:59]\n"
    "ds_read_b64_tr_b16 a[122:123], v201 offset:41984\n"
    "s_barrier\n"
    "v_mfma_f32_16x16x32_bf16 v[60:63], a[108:111], a[152:155], v[60:63]\n"
    "s_waitcnt lgkmcnt(6)\n"
    "v_mfma_f32_16x16x32_bf16 v[64:67], a[112:115], a[144:147], v[64:67]\n"
    "ds_read_b64_tr_b16 a[124:125], v194 offset:49152\n"
    "ds_read_b128 a[200:203], v232 offset:10240\n"
    "v_mfma_f32_16x16x32_bf16 v[68:71], a[112:115], a[148:151], v[68:71]\n"
    "ds_read_b64_tr_b16 a[126:127], v194 offset:50176\n"
    "v_mfma_f32_16x16x32_bf16 v[72:75], a[112:115], a[152:155], v[72:75]\n"
    "s_waitcnt lgkmcnt(6)\n"
    "v_mfma_f32_16x16x32_bf16 v[76:79], a[116:119], a[144:147], v[76:79]\n"
    "ds_read_b64_tr_b16 a[128:129], v195 offset:49152\n"
    "v_mfma_f32_16x16x32_bf16 v[80:83], a[116:119], a[148:151], v[80:83]\n"
    "ds_read_b64_tr_b16 a[130:131], v195 offset:50176\n"
    "v_mfma_f32_16x16x32_bf16 v[84:87], a[116:119], a[152:155], v[84:87]\n"
    "s_waitcnt lgkmcnt(5)\n"
    "v_mfma_f32_16x16x32_bf16 v[88:91], a[120:123], a[144:147], v[88:91]\n"
    "ds_read_b64_tr_b16 a[108:109], v196 offset:49152\n"
    "v_mfma_f32_16x16x32_bf16 v[92:95], a[120:123], a[148:151], v[92:95]\n"
    "ds_read_b64_tr_b16 a[110:111], v196 offset:50176\n"
    "v_mfma_f32_16x16x32_bf16 v[96:99], a[120:123], a[152:155], v[96:99]\n"
    "s_waitcnt lgkmcnt(4)\n"
    "v_mfma_f32_16x16x32_bf16 v[4:7], a[124:127], a[156:159], v[4:7]\n"
    "s_waitcnt vmcnt(9)\n"
    "ds_read_b64_tr_b16 a[112:113], v197 offset:49152\n"
    "v_mfma_f32_16x16x32_bf16 v[8:11], a[124:127], a[160:163], v[8:11]\n"
    "ds_read_b64_tr_b16 a[114:115], v197 offset:50176\n"
    "ds_read_b64 a[228:229], v250 offset:12288\n"
    "v_mfma_f32_16x16x32_bf16 v[12:15], a[124:127], a[164:167], v[12:15]\n"
    "s_waitcnt lgkmcnt(5)\n"
    "ds_read_b64 a[230:231], v251 offset:12288\n"
    "v_mfma_f32_16x16x32_bf16 v[16:19], a[128:131], a[156:159], v[16:19]\n"
    "ds_read_b64_tr_b16 a[116:117], v198 offset:49152\n"
    "ds_read_b64 a[232:233], v250 offset:14336\n"
    "v_mfma_f32_16x16x32_bf16 v[20:23], a[128:131], a[160:163], v[20:23]\n"
    "ds_read_b64_tr_b16 a[118:119], v198 offset:50176\n"
    "ds_read_b64 a[234:235], v251 offset:14336\n"
    "v_mfma_f32_16x16x32_bf16 v[24:27], a[128:131], a[164:167], v[24:27]\n"
    "s_waitcnt lgkmcnt(8)\n"
    "v_mfma_f32_16x16x32_bf16 v[28:31], a[108:111], a[156:159], v[28:31]\n"
    "ds_read_b64_tr_b16 a[120:121], v199 offset:49152\n"
    "ds_read_b64 a[236:237], v250 offset:16384\n"
    "v_mfma_f32_16x16x32_bf16 v[32:35], a[108:111], a[160:163], v[32:35]\n"
    "ds_read_b64_tr_b16 a[122:123], v199 offset:50176\n"
    "ds_read_b64 a[238:239], v251 offset:16384\n"
    "v_mfma_f32_16x16x32_bf16 v[36:39], a[108:111], a[164:167], v[36:39]\n"
    "s_waitcnt lgkmcnt(10)\n"
    "v_mfma_f32_16x16x32_bf16 v[40:43], a[112:115], a[156:159], v[40:43]\n"
    "ds_read_b64_tr_b16 a[124:125], v200 offset:49152\n"
    "ds_read_b64 a[240:241], v252 offset:12288\n"
    "v_mfma_f32_16x16x32_bf16 v[44:47], a[112:115], a[160:163], v[44:47]\n"
    "ds_read_b64_tr_b16 a[126:127], v200 offset:50176\n"
    "ds_read_b64 a[242:243], v253 offset:12288\n"
    "v_mfma_f32_16x16x32_bf16 v[48:51], a[112:115], a[164:167], v[48:51]\n"
    "s_waitcnt lgkmcnt(9)\n"
    "v_mfma_f32_16x16x32_bf16 v[52:55], a[116:119], a[156:159], v[52:55]\n"
    "ds_read_b64_tr_b16 a[128:129], v201 offset:49152\n"
    "ds_read_b64 a[244:245], v252 offset:14336\n"
    "v_mfma_f32_16x16x32_bf16 v[56:59], a[116:119], a[160:163], v[56:59]\n"
    "ds_read_b64_tr_b16 a[130:131], v201 offset:50176\n"
    "ds_read_b64 a[246:247], v253 offset:14336\n"
    "v_mfma_f32_16x16x32_bf16 v[60:63], a[116:119], a[164:167], v[60:63]\n"
    "s_waitcnt lgkmcnt(9)\n"
    "v_mfma_f32_16x16x32_bf16 v[64:67], a[120:123], a[156:159], v[64:67]\n"
    "ds_read_b64_tr_b16 a[108:109], v194 offset:57344\n"
    "ds_read_b64 a[248:249], v252 offset:16384\n"
    "v_mfma_f32_16x16x32_bf16 v[68:71], a[120:123], a[160:163], v[68:71]\n"
    "ds_read_b64_tr_b16 a[110:111], v194 offset:58368\n"
    "ds_read_b64 a[250:251], v253 offset:16384\n"
    "v_mfma_f32_16x16x32_bf16 v[72:75], a[120:123], a[164:167], v[72:75]\n"
    "s_waitcnt lgkmcnt(9)\n"
    "v_mfma_f32_16x16x32_bf16 v[76:79], a[124:127], a[156:159], v[76:79]\n"
    "s_waitcnt lgkmcnt(0)\n"
    "s_add_u32 s63, s65, s98\n"
    "s_add_u32 m0, s60, 12288\n"
    "ds_read_b64_tr_b16 a[112:113], v195 offset:57344\n"
    "v_mfma_f32_16x16x32_bf16 v[80:83], a[124:127], a[160:163], v[80:83]\n"
    "ds_read_b64_tr_b16 a[114:115], v195 offset:58368\n"
    "buffer_load_dwordx4 v220, s[16:19], s63 offen offset:0 lds\n"
    "v_mfma_f32_16x16x32_bf16 v[84:87], a[124:127], a[164:167], v[84:87]\n"
    "v_mfma_f32_16x16x32_bf16 v[88:91], a[128:131], a[156:159], v[88:91]\n"
    "ds_read_b64_tr_b16 a[116:117], v196 offset:57344\n"
    "buffer_load_dwordx4 v221, s[16:19], s63 offen offset:1024 lds\n"
    "v_mfma_f32_16x16x32_bf16 v[92:95], a[128:131], a[160:163], v[92:95]\n"
    "ds_read_b64_tr_b16 a[118:119], v196 offset:58368\n"
    "v_mfma_f32_16x16x32_bf16 v[96:99], a[128:131], a[164:167], v[96:99]\n"
    "v_mfma_f32_16x16x32_bf16 v[4:7], a[108:111], a[168:171], v[4:7]\n"
    "ds_read_b64_tr_b16 a[120:121], v197 offset:57344\n"
    "buffer_load_dwordx4 v222, s[16:19], s63 offen offset:2048 lds\n"
    "v_mfma_f32_16x16x32_bf16 v[8:11], a[108:111], a[172:175], v[8:11]\n"
    "s_add_u32 m0, s60, 15360\n"
    "ds_read_b64_tr_b16 a[122:123], v197 offset:58368\n"
    "v_mfma_f32_16x16x32_bf16 v[12:15], a[108:111], a[176:179], v[12:15]\n"
    "s_waitcnt lgkmcnt(4)\n"
    "v_mfma_f32_16x16x32_bf16 v[16:19], a[112:115], a[168:171], v[16:19]\n"
    "ds_read_b64_tr_b16 a[124:125], v198 offset:57344\n"
    "buffer_load_dwordx4 v223, s[16:19], s63 offen offset:0 lds\n"
    "v_mfma_f32_16x16x32_bf16 v[20:23], a[112:115], a[172:175], v[20:23]\n"
    "ds_read_b64_tr_b16 a[126:127], v198 offset:58368\n"
    "v_mfma_f32_16x16x32_bf16 v[24:27], a[112:115], a[176:179], v[24:27]\n"
    "s_waitcnt lgkmcnt(4)\n"
    "v_mfma_f32_16x16x32_bf16 v[28:31], a[116:119], a[168:171], v[28:31]\n"
    "ds_read_b64_tr_b16 a[128:129], v199 offset:57344\n"
    "buffer_load_dwordx4 v224, s[16:19], s63 offen offset:1024 lds\n"
    "v_mfma_f32_16x16x32_bf16 v[32:35], a[116:119], a[172:175], v[32:35]\n"
    "ds_read_b64_tr_b16 a[130:131], v199 offset:58368\n"
    "v_mfma_f32_16x16x32_bf16 v[36:39], a[116:119], a[176:179], v[36:39]\n"
    "s_waitcnt lgkmcnt(4)\n"
    "v_mfma_f32_16x16x32_bf16 v[40:43], a[120:123], a[168:171], v[40:43]\n"
    "ds_read_b64_tr_b16 a[108:109], v200 offset:57344\n"
    "buffer_load_dwordx4 v225, s[16:19], s63 offen offset:2048 lds\n"
    "v_mfma_f32_16x16x32_bf16 v[44:47], a[120:123], a[172:175], v[44:47]\n"
    "ds_read_b64_tr_b16 a[110:111], v200 offset:58368\n"
    "v_mfma_f32_16x16x32_bf16 v[48:51], a[120:123], a[176:179], v[48:51]\n"
    "s_waitcnt lgkmcnt(4)\n"
    "v_mfma_f32_16x16x32_bf16 v[52:55], a[124:127], a[168:171], v[52:55]\n"
    "ds_read_b64_tr_b16 a[112:113], v201 offset:57344\n"
    "v_mfma_f32_16x16x32_bf16 v[56:59], a[124:127], a[172:175], v[56:59]\n"
    "ds_read_b64_tr_b16 a[114:115], v201 offset:58368\n"
    "s_waitcnt vmcnt(11)\n"
    "s_barrier\n"
    "v_mfma_f32_16x16x32_bf16 v[60:63], a[124:127], a[176:179], v[60:63]\n"
    "s_waitcnt lgkmcnt(4)\n"
    "v_mfma_f32_16x16x32_bf16 v[64:67], a[128:131], a[168:171], v[64:67]\n"
    "ds_read_b64_tr_b16 a[116:117], v194 offset:0\n"
    "v_mfma_f32_16x16x32_bf16 v[68:71], a[128:131], a[172:175], v[68:71]\n"
    "ds_read_b64_tr_b16 a[118:119], v194 offset:1024\n"
    "v_mfma_f32_16x16x32_bf16 v[72:75], a[128:131], a[176:179], v[72:75]\n"
    "s_waitcnt lgkmcnt(4)\n"
    "v_mfma_f32_16x16x32_bf16 v[76:79], a[108:111], a[168:171], v[76:79]\n"
    "ds_read_b64_tr_b16 a[120:121], v195 offset:0\n"
    "v_mfma_f32_16x16x32_bf16 v[80:83], a[108:111], a[172:175], v[80:83]\n"
    "ds_read_b64_tr_b16 a[122:123], v195 offset:1024\n"
    "v_mfma_f32_16x16x32_bf16 v[84:87], a[108:111], a[176:179], v[84:87]\n"
    "s_waitcnt lgkmcnt(4)\n"
    "v_mfma_f32_16x16x32_bf16 v[88:91], a[112:115], a[168:171], v[88:91]\n"
    "ds_read_b64_tr_b16 a[124:125], v196 offset:0\n"
    "v_mfma_f32_16x16x32_bf16 v[92:95], a[112:115], a[172:175], v[92:95]\n"
    "ds_read_b64_tr_b16 a[126:127], v196 offset:1024\n"
    "v_mfma_f32_16x16x32_bf16 v[96:99], a[112:115], a[176:179], v[96:99]\n"
    "s_waitcnt lgkmcnt(4)\n"
    "v_mfma_f32_16x16x32_bf16 v[4:7], a[116:119], a[180:183], v[4:7]\n"
    "s_add_u32 s64, s69, s47\n"
    "ds_read_b64_tr_b16 a[128:129], v197 offset:0\n"
    "v_mfma_f32_16x16x32_bf16 v[8:11], a[116:119], a[184:187], v[8:11]\n"
    "s_add_u32 m0, s50, 20480\n"
    "ds_read_b64_tr_b16 a[130:131], v197 offset:1024\n"
    "v_mfma_f32_16x16x32_bf16 v[12:15], a[116:119], a[188:191], v[12:15]\n"
    "s_waitcnt lgkmcnt(4)\n"
    "buffer_load_dwordx4 v226, s[24:27], s64 offen offset:0 lds\n"
    "v_mfma_f32_16x16x32_bf16 v[16:19], a[120:123], a[180:183], v[16:19]\n"
    "ds_read_b64_tr_b16 a[108:109], v198 offset:0\n"
    "buffer_load_dwordx4 v227, s[24:27], s64 offen offset:1024 lds\n"
    "v_mfma_f32_16x16x32_bf16 v[20:23], a[120:123], a[184:187], v[20:23]\n"
    "ds_read_b64_tr_b16 a[110:111], v198 offset:1024\n"
    "v_mfma_f32_16x16x32_bf16 v[24:27], a[120:123], a[188:191], v[24:27]\n"
    "s_waitcnt lgkmcnt(4)\n"
    "v_mfma_f32_16x16x32_bf16 v[28:31], a[124:127], a[180:183], v[28:31]\n"
    "ds_read_b64_tr_b16 a[112:113], v199 offset:0\n"
    "v_mfma_f32_16x16x32_bf16 v[32:35], a[124:127], a[184:187], v[32:35]\n"
    "ds_read_b64_tr_b16 a[114:115], v199 offset:1024\n"
    "buffer_load_dwordx4 v228, s[24:27], s64 offen offset:2048 lds\n"
    "v_mfma_f32_16x16x32_bf16 v[36:39], a[124:127], a[188:191], v[36:39]\n"
    "s_waitcnt lgkmcnt(4)\n"
    "s_add_u32 m0, s50, 23552\n"
    "v_mfma_f32_16x16x32_bf16 v[40:43], a[128:131], a[180:183], v[40:43]\n"
    "ds_read_b64_tr_b16 a[116:117], v200 offset:0\n"
    "v_mfma_f32_16x16x32_bf16 v[44:47], a[128:131], a[184:187], v[44:47]\n"
    "ds_read_b64_tr_b16 a[118:119], v200 offset:1024\n"
    "buffer_load_dwordx4 v229, s[24:27], s64 offen offset:0 lds\n"
    "v_mfma_f32_16x16x32_bf16 v[48:51], a[128:131], a[188:191], v[48:51]\n"
    "s_waitcnt lgkmcnt(4)\n"
    "v_mfma_f32_16x16x32_bf16 v[52:55], a[108:111], a[180:183], v[52:55]\n"
    "ds_read_b64_tr_b16 a[120:121], v201 offset:0\n"
    "v_mfma_f32_16x16x32_bf16 v[56:59], a[108:111], a[184:187], v[56:59]\n"
    "ds_read_b64_tr_b16 a[122:123], v201 offset:1024\n"
    "v_mfma_f32_16x16x32_bf16 v[60:63], a[108:111], a[188:191], v[60:63]\n"
    "s_waitcnt lgkmcnt(4)\n"
    "buffer_load_dwordx4 v230, s[24:27], s64 offen offset:1024 lds\n"
    "v_mfma_f32_16x16x32_bf16 v[64:67], a[112:115], a[180:183], v[64:67]\n"
    "ds_read_b64_tr_b16 a[124:125], v194 offset:8192\n"
    "v_mfma_f32_16x16x32_bf16 v[68:71], a[112:115], a[184:187], v[68:71]\n"
    "ds_read_b64_tr_b16 a[126:127], v194 offset:9216\n"
    "v_mfma_f32_16x16x32_bf16 v[72:75], a[112:115], a[188:191], v[72:75]\n"
    "s_waitcnt lgkmcnt(4)\n"
    "v_mfma_f32_16x16x32_bf16 v[76:79], a[116:119], a[180:183], v[76:79]\n"
    "ds_read_b64_tr_b16 a[128:129], v195 offset:8192\n"
    "v_mfma_f32_16x16x32_bf16 v[80:83], a[116:119], a[184:187], v[80:83]\n"
    "ds_read_b64_tr_b16 a[130:131], v195 offset:9216\n"
    "v_mfma_f32_16x16x32_bf16 v[84:87], a[116:119], a[188:191], v[84:87]\n"
    "s_waitcnt lgkmcnt(4)\n"
    "v_mfma_f32_16x16x32_bf16 v[88:91], a[120:123], a[180:183], v[88:91]\n"
    "ds_read_b64_tr_b16 a[108:109], v196 offset:8192\n"
    "v_mfma_f32_16x16x32_bf16 v[92:95], a[120:123], a[184:187], v[92:95]\n"
    "ds_read_b64_tr_b16 a[110:111], v196 offset:9216\n"
    "v_mfma_f32_16x16x32_bf16 v[96:99], a[120:123], a[188:191], v[96:99]\n"
    "s_waitcnt lgkmcnt(4)\n"
    "v_mfma_f32_16x16x32_bf16 v[4:7], a[124:127], a[192:195], v[4:7]\n"
    "ds_read_b64_tr_b16 a[112:113], v197 offset:8192\n"
    "v_mfma_f32_16x16x32_bf16 v[8:11], a[124:127], a[196:199], v[8:11]\n"
    "ds_read_b64_tr_b16 a[114:115], v197 offset:9216\n"
    "v_mfma_f32_16x16x32_bf16 v[12:15], a[124:127], a[200:203], v[12:15]\n"
    "s_waitcnt lgkmcnt(4)\n"
    "v_mfma_f32_16x16x32_bf16 v[16:19], a[128:131], a[192:195], v[16:19]\n"
    "ds_read_b64_tr_b16 a[116:117], v198 offset:8192\n"
    "v_mfma_f32_16x16x32_bf16 v[20:23], a[128:131], a[196:199], v[20:23]\n"
    "ds_read_b64_tr_b16 a[118:119], v198 offset:9216\n"
    "v_mfma_f32_16x16x32_bf16 v[24:27], a[128:131], a[200:203], v[24:27]\n"
    "s_waitcnt lgkmcnt(4)\n"
    "v_mfma_f32_16x16x32_bf16 v[28:31], a[108:111], a[192:195], v[28:31]\n"
    "ds_read_b64_tr_b16 a[120:121], v199 offset:8192\n"
    "v_mfma_f32_16x16x32_bf16 v[32:35], a[108:111], a[196:199], v[32:35]\n"
    "ds_read_b64_tr_b16 a[122:123], v199 offset:9216\n"
    "v_cvt_pk_bf16_f32 v112, v4, v5\n"
    "v_mfma_f32_16x16x32_bf16 v[36:39], a[108:111], a[200:203], v[36:39]\n"
    "s_waitcnt lgkmcnt(4)\n"
    "v_cvt_pk_bf16_f32 v113, v6, v7\n"
    "v_cvt_pk_bf16_f32 v114, v16, v17\n"
    "v_mfma_f32_16x16x32_bf16 v[40:43], a[112:115], a[192:195], v[40:43]\n"
    "ds_read_b64_tr_b16 a[124:125], v200 offset:8192\n"
    "v_cvt_pk_bf16_f32 v115, v18, v19\n"
    "v_mfma_f32_16x16x32_bf16 v[44:47], a[112:115], a[196:199], v[44:47]\n"
    "ds_read_b64_tr_b16 a[126:127], v200 offset:9216\n"
    "v_cvt_pk_bf16_f32 v116, v8, v9\n"
    "v_mfma_f32_16x16x32_bf16 v[48:51], a[112:115], a[200:203], v[48:51]\n"
    "s_waitcnt lgkmcnt(4)\n"
    "v_cvt_pk_bf16_f32 v117, v10, v11\n"
    "v_cvt_pk_bf16_f32 v118, v20, v21\n"
    "v_mfma_f32_16x16x32_bf16 v[52:55], a[116:119], a[192:195], v[52:55]\n"
    "ds_read_b64_tr_b16 a[128:129], v201 offset:8192\n"
    "v_cvt_pk_bf16_f32 v119, v22, v23\n"
    "v_mfma_f32_16x16x32_bf16 v[56:59], a[116:119], a[196:199], v[56:59]\n"
    "ds_read_b64_tr_b16 a[130:131], v201 offset:9216\n"
    "s_barrier\n"
    "v_cvt_pk_bf16_f32 v120, v12, v13\n"
    "v_mfma_f32_16x16x32_bf16 v[60:63], a[116:119], a[200:203], v[60:63]\n"
    "s_waitcnt lgkmcnt(4)\n"
    "v_cvt_pk_bf16_f32 v121, v14, v15\n"
    "v_cvt_pk_bf16_f32 v122, v24, v25\n"
    "v_mfma_f32_16x16x32_bf16 v[64:67], a[120:123], a[192:195], v[64:67]\n"
    "v_cvt_pk_bf16_f32 v123, v26, v27\n"
    "v_cvt_pk_bf16_f32 v124, v28, v29\n"
    "v_mfma_f32_16x16x32_bf16 v[68:71], a[120:123], a[196:199], v[68:71]\n"
    "v_cvt_pk_bf16_f32 v125, v30, v31\n"
    "v_cvt_pk_bf16_f32 v126, v40, v41\n"
    "v_mfma_f32_16x16x32_bf16 v[72:75], a[120:123], a[200:203], v[72:75]\n"
    "s_waitcnt lgkmcnt(2)\n"
    "v_cvt_pk_bf16_f32 v127, v42, v43\n"
    "v_cvt_pk_bf16_f32 v128, v32, v33\n"
    "v_mfma_f32_16x16x32_bf16 v[76:79], a[124:127], a[192:195], v[76:79]\n"
    "v_cvt_pk_bf16_f32 v129, v34, v35\n"
    "v_cvt_pk_bf16_f32 v130, v44, v45\n"
    "v_mfma_f32_16x16x32_bf16 v[80:83], a[124:127], a[196:199], v[80:83]\n"
    "v_cvt_pk_bf16_f32 v131, v46, v47\n"
    "v_cvt_pk_bf16_f32 v132, v36, v37\n"
    "v_mfma_f32_16x16x32_bf16 v[84:87], a[124:127], a[200:203], v[84:87]\n"
    "s_waitcnt lgkmcnt(0)\n"
    "v_cvt_pk_bf16_f32 v133, v38, v39\n"
    "v_cvt_pk_bf16_f32 v134, v48, v49\n"
    "v_mfma_f32_16x16x32_bf16 v[88:91], a[128:131], a[192:195], v[88:91]\n"
    "v_cvt_pk_bf16_f32 v135, v50, v51\n"
    "v_cvt_pk_bf16_f32 v136, v52, v53\n"
    "v_mfma_f32_16x16x32_bf16 v[92:95], a[128:131], a[196:199], v[92:95]\n"
    "v_cvt_pk_bf16_f32 v137, v54, v55\n"
    "v_cvt_pk_bf16_f32 v138, v64, v65\n"
    "v_mfma_f32_16x16x32_bf16 v[96:99], a[128:131], a[200:203], v[96:99]\n"
    "v_cvt_pk_bf16_f32 v139, v66, v67\n"
    "v_cvt_pk_bf16_f32 v140, v56, v57\n"
    "v_mfma_f32_16x16x32_bf16 v[160:163], a[204:207], v[112:115], 0\n"
    "v_cvt_pk_bf16_f32 v141, v58, v59\n"
    "v_cvt_pk_bf16_f32 v142, v68, v69\n"
    "v_mfma_f32_16x16x32_bf16 v[164:167], a[208:211], v[112:115], 0\n"
    "v_cvt_pk_bf16_f32 v143, v70, v71\n"
    "v_cvt_pk_bf16_f32 v144, v60, v61\n"
    "v_mfma_f32_16x16x32_bf16 v[168:171], a[212:215], v[112:115], 0\n"
    "s_add_u32 s63, s65, s99\n"
    "v_cvt_pk_bf16_f32 v145, v62, v63\n"
    "v_cvt_pk_bf16_f32 v146, v72, v73\n"
    "v_mfma_f32_16x16x32_bf16 v[172:175], a[204:207], v[116:119], 0\n"
    "s_add_u32 m0, s60, 0\n"
    "s_nop 0\n"
    "buffer_load_dwordx4 v220, s[16:19], s63 offen offset:0 lds\n"
    "v_cvt_pk_bf16_f32 v147, v74, v75\n"
    "v_mfma_f32_16x16x32_bf16 v[176:179], a[208:211], v[116:119], 0\n"
    "buffer_load_dwordx4 v221, s[16:19], s63 offen offset:1024 lds\n"
    "v_cvt_pk_bf16_f32 v148, v76, v77\n"
    "v_mfma_f32_16x16x32_bf16 v[180:183], a[212:215], v[116:119], 0\n"
    "buffer_load_dwordx4 v222, s[16:19], s63 offen offset:2048 lds\n"
    "v_cvt_pk_bf16_f32 v149, v78, v79\n"
    "v_mfma_f32_16x16x32_bf16 v[192:195], a[204:207], v[120:123], 0\n"
    "s_add_u32 m0, s60, 3072\n"
    "s_nop 0\n"
    "buffer_load_dwordx4 v223, s[16:19], s63 offen offset:0 lds\n"
    "v_cvt_pk_bf16_f32 v150, v88, v89\n"
    "v_mfma_f32_16x16x32_bf16 v[204:207], a[208:211], v[120:123], 0\n"
    "v_cvt_pk_bf16_f32 v151, v90, v91\n"
    "v_cvt_pk_bf16_f32 v152, v80, v81\n"
    "v_mfma_f32_16x16x32_bf16 v[216:219], a[212:215], v[120:123], 0\n"
    "buffer_load_dwordx4 v224, s[16:19], s63 offen offset:1024 lds\n"
    "v_cvt_pk_bf16_f32 v153, v82, v83\n"
    "v_mfma_f32_16x16x32_bf16 v[160:163], a[216:219], v[124:127], v[160:163]\n"
    "v_cvt_pk_bf16_f32 v154, v92, v93\n"
    "v_cvt_pk_bf16_f32 v155, v94, v95\n"
    "v_mfma_f32_16x16x32_bf16 v[164:167], a[220:223], v[124:127], v[164:167]\n"
    "buffer_load_dwordx4 v225, s[16:19], s63 offen offset:2048 lds\n"
    "v_cvt_pk_bf16_f32 v156, v84, v85\n"
    "v_mfma_f32_16x16x32_bf16 v[168:171], a[224:227], v[124:127], v[168:171]\n"
    "v_cvt_pk_bf16_f32 v157, v86, v87\n"
    "v_cvt_pk_bf16_f32 v158, v96, v97\n"
    "v_mfma_f32_16x16x32_bf16 v[172:175], a[216:219], v[128:131], v[172:175]\n"
    "s_waitcnt vmcnt(11)\n"
    "ds_read_b128 a[132:135], v231 offset:12288\n"
    "v_cvt_pk_bf16_f32 v159, v98, v99\n"
    "s_branch L_join_%=\n"
    "L_f1_slow_%=:\n"
    "s_mov_b32 s82, 0\n"
    "L_f1_loop_%=:\n"
    "s_sleep 1\n"
    "buffer_load_dword v248, v241, s[28:31], 0 offen sc1\n"
    "s_waitcnt vmcnt(0)\n"
    "v_cmp_ne_u32 vcc, v249, v248\n"
    "s_nop 4\n"
    "s_cbranch_vccz L_f1_ret_%=\n"
    "s_add_u32 s82, s82, 1\n"
    "s_cmp_lt_u32 s82, 200000\n"
    "s_cbranch_scc1 L_f1_loop_%=\n"
    "s_branch L_f1_ret_%=\n"
    "L_own0_%=:\n"
    "v_accvgpr_read_b32 v4, a0\n"
    "v_accvgpr_read_b32 v5, a1\n"
    "v_accvgpr_read_b32 v6, a2\n"
    "v_accvgpr_read_b32 v7, a3\n"
    "v_accvgpr_read_b32 v8, a4\n"
    "v_accvgpr_read_b32 v9, a5\n"
    "v_accvgpr_read_b32 v10, a6\n"
    "v_accvgpr_read_b32 v11, a7\n"
    "v_accvgpr_read_b32 v12, a80\n"
    "v_accvgpr_read_b32 v13, a81\n"
    "v_accvgpr_read_b32 v14, a82\n"
    "v_accvgpr_read_b32 v15, a83\n"
    "v_accvgpr_read_b32 v16, a84\n"
    "v_accvgpr_read_b32 v17, a85\n"
    "v_accvgpr_read_b32 v18, a86\n"
    "v_accvgpr_read_b32 v19, a87\n"
    "s_branch L_ownret_%=\n"
    "L_own1_%=:\n"
    "v_accvgpr_read_b32 v4, a8\n"
    "v_accvgpr_read_b32 v5, a9\n"
    "v_accvgpr_read_b32 v6, a10\n"
    "v_accvgpr_read_b32 v7, a11\n"
    "v_accvgpr_read_b32 v8, a12\n"
    "v_accvgpr_read_b32 v9, a13\n"
    "v_accvgpr_read_b32 v10, a14\n"
    "v_accvgpr_read_b32 v11, a15\n"
    "v_accvgpr_read_b32 v12, a88\n"
    "v_accvgpr_read_b32 v13, a89\n"
    "v_accvgpr_read_b32 v14, a90\n"
    "v_accvgpr_read_b32 v15, a91\n"
    "v_accvgpr_read_b32 v16, a92\n"
    "v_accvgpr_read_b32 v17, a93\n"
    "v_accvgpr_read_b32 v18, a94\n"
    "v_accvgpr_read_b32 v19, a95\n"
    "s_branch L_ownret_%=\n"
    "L_own2_%=:\n"
    "v_accvgpr_read_b32 v4, a16\n"
    "v_accvgpr_read_b32 v5, a17\n"
    "v_accvgpr_read_b32 v6, a18\n"
    "v_accvgpr_read_b32 v7, a19\n"
    "v_accvgpr_read_b32 v8, a20\n"
    "v_accvgpr_read_b32 v9, a21\n"
    "v_accvgpr_read_b32 v10, a22\n"
    "v_accvgpr_read_b32 v11, a23\n"
    "v_accvgpr_read_b32 v12, a96\n"
    "v_accvgpr_read_b32 v13, a97\n"
    "v_accvgpr_read_b32 v14, a98\n"
    "v_accvgpr_read_b32 v15, a99\n"
    "v_accvgpr_read_b32 v16, a100\n"
    "v_accvgpr_read_b32 v17, a101\n"
    "v_accvgpr_read_b32 v18, a102\n"
    "v_accvgpr_read_b32 v19, a103\n"
    "s_branch L_ownret_%=\n"
    "L_own3_%=:\n"
    "v_accvgpr_read_b32 v4, a24\n"
    "v_accvgpr_read_b32 v5, a25\n"
    "v_accvgpr_read_b32 v6, a26\n"
    "v_accvgpr_read_b32 v7, a27\n"
    "v_accvgpr_read_b32 v8, a28\n"
    "v_accvgpr_read_b32 v9, a29\n"
    "v_accvgpr_read_b32 v10, a30\n"
    "v_accvgpr_read_b32 v11, a31\n"
    "v_accvgpr_read_b32 v12, a104\n"
    "v_accvgpr_read_b32 v13, a105\n"
    "v_accvgpr_read_b32 v14, a106\n"
    "v_accvgpr_read_b32 v15, a107\n"
    "v_mov_b32 v16, 0\n"
    "v_mov_b32 v17, 0\n"
    "v_mov_b32 v18, 0\n"
    "v_mov_b32 v19, 0\n"
    "s_branch L_ownret_%=\n"
    "L_own4_%=:\n"
    "v_accvgpr_read_b32 v4, a32\n"
    "v_accvgpr_read_b32 v5, a33\n"
    "v_accvgpr_read_b32 v6, a34\n"
    "v_accvgpr_read_b32 v7, a35\n"
    "v_accvgpr_read_b32 v8, a36\n"
    "v_accvgpr_read_b32 v9, a37\n"
    "v_accvgpr_read_b32 v10, a38\n"
    "v_accvgpr_read_b32 v11, a39\n"
    "v_accvgpr_read_b32 v12, a48\n"
    "v_accvgpr_read_b32 v13, a49\n"
    "v_accvgpr_read_b32 v14, a50\n"
    "v_accvgpr_read_b32 v15, a51\n"
    "v_accvgpr_read_b32 v16, a52\n"
    "v_accvgpr_read_b32 v17, a53\n"
    "v_accvgpr_read_b32 v18, a54\n"
    "v_accvgpr_read_b32 v19, a55\n"
    "s_branch L_ownret_%=\n"
    "L_own5_%=:\n"
    "v_accvgpr_read_b32 v4, a40\n"
    "v_accvgpr_read_b32 v5, a41\n"
    "v_accvgpr_read_b32 v6, a42\n"
    "v_accvgpr_read_b32 v7, a43\n"
    "v_accvgpr_read_b32 v8, a44\n"
    "v_accvgpr_read_b32 v9, a45\n"
    "v_accvgpr_read_b32 v10, a46\n"
    "v_accvgpr_read_b32 v11, a47\n"
    "v_accvgpr_read_b32 v12, a56\n"
    "v_accvgpr_read_b32 v13, a57\n"
    "v_accvgpr_read_b32 v14, a58\n"
    "v_accvgpr_read_b32 v15, a59\n"
    "v_accvgpr_read_b32 v16, a60\n"
    "v_accvgpr_read_b32 v17, a61\n"
    "v_accvgpr_read_b32 v18, a62\n"
    "v_accvgpr_read_b32 v19, a63\n"
    "s_branch L_ownret_%=\n"
    "L_own6_%=:\n"
    "v_accvgpr_read_b32 v12, a64\n"
    "v_accvgpr_read_b32 v13, a65\n"
    "v_accvgpr_read_b32 v14, a66\n"
    "v_accvgpr_read_b32 v15, a67\n"
    "v_accvgpr_read_b32 v16, a68\n"
    "v_accvgpr_read_b32 v17, a69\n"
    "v_accvgpr_read_b32 v18, a70\n"
    "v_accvgpr_read_b32 v19, a71\n"
    "s_branch L_ownret_%=\n"
    "L_own7_%=:\n"
    "v_accvgpr_read_b32 v12, a72\n"
    "v_accvgpr_read_b32 v13, a73\n"
    "v_accvgpr_read_b32 v14, a74\n"
    "v_accvgpr_read_b32 v15, a75\n"
    "v_accvgpr_read_b32 v16, a76\n"
    "v_accvgpr_read_b32 v17, a77\n"
    "v_accvgpr_read_b32 v18, a78\n"
    "v_accvgpr_read_b32 v19, a79\n"
    "s_branch L_ownret_%=\n"
    "L_r0_slow_%=:\n"
    "s_mov_b32 s82, 0\n"
    "L_r0_loop_%=:\n"
    "s_sleep 1\n"
    "buffer_load_dword v248, v242, s[28:31], 0 offen sc1\n"
    "s_waitcnt vmcnt(0)\n"
    "v_cmp_ne_u32 vcc, v249, v248\n"
    "s_nop 4\n"
    "s_cbranch_vccz L_r0_ret_%=\n"
    "s_add_u32 s82, s82, 1\n"
    "s_cmp_lt_u32 s82, 200000\n"
    "s_cbranch_scc1 L_r0_loop_%=\n"
    "s_branch L_r0_ret_%=\n"
    "L_lateA_%=:\n"
    "s_waitcnt vmcnt(0)\n"
    "s_add_u32 s81, s59, 397312\n"
    "buffer_store_dword v249, v240, s[28:31], s81 offen offset:0 sc1\n"
    "s_branch L_r0_slow_%=\n"
    "L_r0_ret_%=:\n"
    "buffer_load_dwordx4 v[188:191], v237, s[28:31], s78 offen offset:0 sc1\n"
    "buffer_load_dwordx4 v[192:195], v237, s[28:31], s78 offen offset:1024 sc1\n"
    "buffer_load_dwordx4 v[200:203], v237, s[28:31], s78 offen offset:2048 sc1\n"
    "buffer_load_dwordx4 v[204:207], v237, s[28:31], s78 offen offset:3072 sc1\n"
    "buffer_load_dwordx4 v[212:215], v239, s[28:31], s78 offen offset:0 sc1\n"
    "buffer_load_dwordx4 v[216:219], v239, s[28:31], s78 offen offset:1024 sc1\n"
    "buffer_load_dwordx4 v[96:99], v239, s[28:31], s78 offen offset:2048 sc1\n"
    "s_branch L_joinA_%=\n"
    "L_r1_slow_%=:\n"
    "s_mov_b32 s82, 0\n"
    "L_r1_loop_%=:\n"
    "s_sleep 1\n"
    "buffer_load_dword v248, v242, s[28:31], s52 offen sc1\n"
    "s_waitcnt vmcnt(0)\n"
    "v_cmp_ne_u32 vcc, v249, v248\n"
    "s_nop 4\n"
    "s_cbranch_vccz L_r1_ret_%=\n"
    "s_add_u32 s82, s82, 1\n"
    "s_cmp_lt_u32 s82, 200000\n"
    "s_cbranch_scc1 L_r1_loop_%=\n"
    "s_branch L_r1_ret_%=\n"
    "L_end_%=:\n"
    "s_endpgm\n"
    :
    : "s"(__builtin_amdgcn_kernarg_segment_ptr()), "s"(blockIdx.x), "v"(threadIdx.x), "s"((unsigned)(size_t)lds)
    : "v1", "v2", "v3", "v4", "v5", "v6", "v7", "v8", "v9", "v10", "v11", "v12", "v13", "v14", "v15", "v16", "v17", "v18", "v19", "v20", "v21", "v22", "v23", "v24",
      "v25", "v26", "v27", "v28", "v29", "v30", "v31", "v32", "v33", "v34", "v35", "v36", "v37", "v38", "v39", "v40", "v41", "v42", "v43", "v44", "v45", "v46", "v47", "v48",
      "v49", "v50", "v51", "v52", "v53", "v54", "v55", "v56", "v57", "v58", "v59", "v60", "v61", "v62", "v63", "v64", "v65", "v66", "v67", "v68", "v69", "v70", "v71", "v72",
      "v73", "v74", "v75", "v76", "v77", "v78", "v79", "v80", "v81", "v82", "v83", "v84", "v85", "v86", "v87", "v88", "v89", "v90", "v91", "v92", "v93", "v94", "v95", "v96",
      "v97", "v98", "v99", "v100", "v101", "v102", "v103", "v104", "v105", "v106", "v107", "v108", "v109", "v110", "v111", "v112", "v113", "v114", "v115", "v116", "v117", "v118", "v119", "v120",
      "v121", "v122", "v123", "v124", "v125", "v126", "v127", "v128", "v129", "v130", "v131", "v132", "v133", "v134", "v135", "v136", "v137", "v138", "v139", "v140", "v141", "v142", "v143", "v144",
      "v145", "v146", "v147", "v148", "v149", "v150", "v151", "v152", "v153", "v154", "v155", "v156", "v157", "v158", "v159", "v160", "v161", "v162", "v163", "v164", "v165", "v166", "v167", "v168",
      "v169", "v170", "v171", "v172", "v173", "v174", "v175", "v176", "v177", "v178", "v179", "v180", "v181", "v182", "v183", "v184", "v185", "v186", "v187", "v188", "v189", "v190", "v191", "v192",
      "v193", "v194", "v195", "v196", "v197", "v198", "v199", "v200", "v201", "v202", "v203", "v204", "v205", "v206", "v207", "v208", "v209", "v210", "v211", "v212", "v213", "v214", "v215", "v216",
      "v217", "v218", "v219", "v220", "v221", "v222", "v223", "v224", "v225", "v226", "v227", "v228", "v229", "v230", "v231", "v232", "v233", "v234", "v235", "v236", "v237", "v238", "v239", "v240",
      "v241", "v242", "v243", "v244", "v245", "v246", "v247", "v248", "v249", "v250", "v251", "v252", "v253", "v254", "v255", "a0", "a1", "a2", "a3", "a4", "a5", "a6", "a7", "a8",
      "a9", "a10", "a11", "a12", "a13", "a14", "a15", "a16", "a17", "a18", "a19", "a20", "a21", "a22", "a23", "a24", "a25", "a26", "a27", "a28", "a29", "a30", "a31", "a32",
      "a33", "a34", "a35", "a36", "a37", "a38", "a39", "a40", "a41", "a42", "a43", "a44", "a45", "a46", "a47", "a48", "a49", "a50", "a51", "a52", "a53", "a54", "a55", "a56",
      "a57", "a58", "a59", "a60", "a61", "a62", "a63", "a64", "a65", "a66", "a67", "a68", "a69", "a70", "a71", "a72", "a73", "a74", "a75", "a76", "a77", "a78", "a79", "a80",
      "a81", "a82", "a83", "a84", "a85", "a86", "a87", "a88", "a89", "a90", "a91", "a92", "a93", "a94", "a95", "a96", "a97", "a98", "a99", "a100", "a101", "a102", "a103", "a104",
      "a105", "a106", "a107", "a108", "a109", "a110", "a111", "a112", "a113", "a114", "a115", "a116", "a117", "a118", "a119", "a120", "a121", "a122", "a123", "a124", "a125", "a126", "a127", "a128",
      "a129", "a130", "a131", "a132", "a133", "a134", "a135", "a136", "a137", "a138", "a139", "a140", "a141", "a142", "a143", "a144", "a145", "a146", "a147", "a148", "a149", "a150", "a151", "a152",
      "a153", "a154", "a155", "a156", "a157", "a158", "a159", "a160", "a161", "a162", "a163", "a164", "a165", "a166", "a167", "a168", "a169", "a170", "a171", "a172", "a173", "a174", "a175", "a176",
      "a177", "a178", "a179", "a180", "a181", "a182", "a183", "a184", "a185", "a186", "a187", "a188", "a189", "a190", "a191", "a192", "a193", "a194", "a195", "a196", "a197", "a198", "a199", "a200",
      "a201", "a202", "a203", "a204", "a205", "a206", "a207", "a208", "a209", "a210", "a211", "a212", "a213", "a214", "a215", "a216", "a217", "a218", "a219", "a220", "a221", "a222", "a223", "a224",
      "a225", "a226", "a227", "a228", "a229", "a230", "a231", "a232", "a233", "a234", "a235", "a236", "a237", "a238", "a239", "a240", "a241", "a242", "a243", "a244", "a245", "a246", "a247", "a248",
      "a249", "a250", "a251", "a252", "a253", "a254", "a255", "s4", "s5", "s6", "s7", "s8", "s9", "s10", "s11", "s12", "s13", "s14", "s15", "s16", "s17", "s18", "s19", "s20",
      "s21", "s22", "s23", "s24", "s25", "s26", "s27", "s28", "s29", "s30", "s31", "s33", "s34", "s35", "s36", "s37", "s38", "s39", "s40", "s41", "s42", "s43", "s44", "s45",
      "s46", "s47", "s48", "s49", "s50", "s51", "s52", "s53", "s54", "s55", "s56", "s57", "s58", "s59", "s60", "s61", "s62", "s63", "s64", "s65", "s66", "s67", "s68", "s69",
      "s70", "s71", "s72", "s73", "s74", "s75", "s76", "s77", "s78", "s79", "s80", "s81", "s82", "s83", "s84", "s85", "s86", "s87", "s88", "s89", "s90", "s91", "s92", "s93",
      "s94", "s95", "s96", "s97", "s98", "s99", "vcc", "scc", "memory");
}

extern "C" void kernel_launch(void* const* d_in, const int* in_sizes, int n_in, void* d_out, int out_size, void* d_ws, size_t ws_size,
                              hipStream_t stream) {
  fused_attn_block_kernel<<<256, 256, 0, stream>>>(d_in[0], d_in[1], d_in[2], d_in[3], d_in[4], d_in[5], d_in[6], d_in[7], d_in[8], d_in[9], d_out, d_ws);
}
```
